```python
import torch

def _softmax(x, axis=-1):
    return torch.softmax(x, dim=axis)
def _gelu(x):
    return torch.nn.functional.gelu(x, approximate="tanh")
def _silu(x):
    return torch.nn.functional.silu(x)
def _sigmoid(x):
    return torch.sigmoid(x)
def _layernorm(x, axis=-1, eps=1e-5):
    m = x.mean(dim=axis, keepdim=True)
    v = x.var(dim=axis, keepdim=True, unbiased=False)
    return (x - m) / torch.sqrt(v + eps)
def _scatter_add(tgt, idx, src, axis=0):
    return tgt.clone().index_add_(axis, idx, src)
def _segment_sum(src, seg, n):
    out = torch.zeros((n,) + src.shape[1:], dtype=src.dtype)
    return out.index_add_(0, seg, src)
def _pad(x, pw):
    flat = tuple(v for pair in reversed(pw) for v in pair)
    return torch.nn.functional.pad(x, flat)

kernel_name = 'gated_mix__sliding_block__spectral_fnet__prompt'

def setup_inputs():

    _w = (lambda a: (lambda t: t.to(torch.bfloat16) if t.is_floating_point() else t)(torch.as_tensor(a)))
    def _A_setup():
        B, S, D = 1, 2048, 512
        return {
            "x": _w((lambda *s: torch.randn(*s, dtype=torch.bfloat16) * 0.05)(B, S, D)),
            "wqkv": _w((lambda *s: torch.randn(*s, dtype=torch.bfloat16) * 1.6)(D, 3 * D)),
            "wo": _w((lambda *s: torch.randn(*s, dtype=torch.bfloat16) * 1.6)(D, D)),
        }
    _A_in = _A_setup()

    def _B_setup():
        B, S, D = 1, 2048, 512
        ns = torch.arange(S)[:, None] * torch.arange(S)[None, :]
        nd = torch.arange(D)[:, None] * torch.arange(D)[None, :]
        Ws_re = torch.cos(-2.0 * torch.pi * ns / S).to(float)
        Ws_im = torch.sin(-2.0 * torch.pi * ns / S).to(float)
        Wd_re = torch.cos(-2.0 * torch.pi * nd / D).to(float)
        Wd_im = torch.sin(-2.0 * torch.pi * nd / D).to(float)
        return {
            "x": _w((lambda *s: torch.randn(*s, dtype=torch.bfloat16) * 0.05)(B, S, D)),
            "dft_seq_re": _w(Ws_re),
            "dft_seq_im": _w(Ws_im),
            "dft_hid_re": _w(Wd_re),
            "dft_hid_im": _w(Wd_im),
        }
    _B_in = _B_setup()

    return {
        "A_x": _A_in["x"],
        "A_wqkv": _A_in["wqkv"],
        "A_wo": _A_in["wo"],
        "B_x": _B_in["x"],
        "B_dft_seq_re": _B_in["dft_seq_re"],
        "B_dft_seq_im": _B_in["dft_seq_im"],
        "B_dft_hid_re": _B_in["dft_hid_re"],
        "B_dft_hid_im": _B_in["dft_hid_im"],
        "gate_w": _w((lambda *s: torch.randn(*s, dtype=torch.bfloat16) * 0.05)(512) * 16.0),
    }

def reference(A_x, A_wqkv, A_wo, B_x, B_dft_seq_re, B_dft_seq_im, B_dft_hid_re, B_dft_hid_im, gate_w):
    def _A_ref(x, wqkv, wo):
        B, S, D = x.shape
        H = 32; Dh = D // H; W = 256
        qkv = (lambda x, *d: x.permute(*d))((x @ wqkv).reshape(B, S, 3, H, Dh), 2, 0, 3, 1, 4)
        q, k, v = qkv[0], qkv[1], qkv[2]
        scores = torch.einsum("bhqd,bhkd->bhqk", q, k) * (Dh ** -0.5)
        idx = torch.arange(S)
        mask = (idx[:, None] - idx[None, :] >= 0) & (idx[:, None] - idx[None, :] < W)
        scores = torch.where(mask[None, None], scores, -1e9)
        p = _softmax(scores, axis=-1)
        o = torch.einsum("bhqk,bhkd->bhqd", p, v)
        return (lambda x, *d: x.permute(*d))(o, 0, 2, 1, 3).reshape(B, S, D) @ wo

    def _B_ref(x, dft_seq_re, dft_seq_im, dft_hid_re, dft_hid_im):
        a = torch.einsum("bsd,ed->bse", x, dft_hid_re)
        b = torch.einsum("bsd,ed->bse", x, dft_hid_im)
        y = torch.einsum("ts,bse->bte", dft_seq_re, a) - torch.einsum("ts,bse->bte", dft_seq_im, b)
        return y

    x = A_x
    g = _sigmoid((x * gate_w).sum(-1, keepdims=True))
    a = _A_ref(x, A_wqkv, A_wo)
    b = _B_ref(x, B_dft_seq_re, B_dft_seq_im, B_dft_hid_re, B_dft_hid_im)
    return g * a + (1 - g) * b
```

```cpp
#include <hip/hip_runtime.h>
#include <hip/hip_bf16.h>
typedef unsigned short u16;
__device__ __forceinline__ float bf2f(u16 v){ return __uint_as_float(((unsigned)v)<<16); }
__device__ __forceinline__ u16 f2bf(float f){ unsigned u=__float_as_uint(f); u += 0x7fffu + ((u>>16)&1u); return (u16)(u>>16); }
constexpr int S=2048, D=512, H=32, DH=16, W=256;

template<bool BT>
__global__ void gemm_naive(const u16* A, const u16* B, u16* C, int M, int N, int K, int ldb, int ldc){
  int n = blockIdx.x*64 + (threadIdx.x&63);
  int m = blockIdx.y*4 + (threadIdx.x>>6);
  float acc=0.f;
  for(int k=0;k<K;++k){
    float b = BT ? bf2f(B[(long)n*ldb+k]) : bf2f(B[(long)k*ldb+n]);
    acc += bf2f(A[(long)m*K+k])*b;
  }
  C[(long)m*ldc+n]=f2bf(acc);
}
__global__ void attn_naive(const u16* qkv, u16* o){
  int q = blockIdx.x*64 + (threadIdx.x&63);
  int h = blockIdx.y;
  float qv[DH];
  for(int d=0;d<DH;++d) qv[d]=bf2f(qkv[(long)q*3*D + h*DH + d]);
  int k0 = q-W+1; if(k0<0) k0=0;
  float m=-1e30f;
  for(int k=k0;k<=q;++k){ float s=0; for(int d=0;d<DH;++d) s+=qv[d]*bf2f(qkv[(long)k*3*D + D + h*DH + d]); s*=0.25f; m=fmaxf(m,s);}
  float l=0; float acc[DH]; for(int d=0;d<DH;++d) acc[d]=0;
  for(int k=k0;k<=q;++k){ float s=0; for(int d=0;d<DH;++d) s+=qv[d]*bf2f(qkv[(long)k*3*D + D + h*DH + d]); s*=0.25f; float p=__expf(s-m); l+=p;
    for(int d=0;d<DH;++d) acc[d]+=p*bf2f(qkv[(long)k*3*D + 2*D + h*DH + d]); }
  for(int d=0;d<DH;++d) o[(long)q*D + h*DH + d]=f2bf(acc[d]/l);
}
__global__ void mix_naive(const u16* x, const u16* gw, const u16* a, const u16* y1, const u16* y2, u16* out){
  int row = blockIdx.x; int t=threadIdx.x;
  float s=0; for(int i=t;i<D;i+=64) s+=bf2f(x[(long)row*D+i])*bf2f(gw[i]);
  for(int o=32;o>0;o>>=1) s+=__shfl_xor(s,o);
  float g=1.f/(1.f+__expf(-s));
  for(int i=t;i<D;i+=64){ long idx=(long)row*D+i; float b=bf2f(y1[idx])-bf2f(y2[idx]); out[idx]=f2bf(g*bf2f(a[idx])+(1.f-g)*b);}
}
extern "C" void kernel_launch(void* const* d_in, const int* in_sizes, int n_in,
                              void* d_out, int out_size, void* d_ws, size_t ws_size,
                              hipStream_t stream) {
  const u16* x=(const u16*)d_in[0]; const u16* wqkv=(const u16*)d_in[1]; const u16* wo=(const u16*)d_in[2];
  const u16* cre=(const u16*)d_in[4]; const u16* cim=(const u16*)d_in[5]; const u16* hre=(const u16*)d_in[6]; const u16* him=(const u16*)d_in[7];
  const u16* gw=(const u16*)d_in[8];
  u16* ws=(u16*)d_ws;
  u16* qkv=ws; u16* o=qkv+S*3*D; u16* a=o+S*D; u16* b=a+S*D; u16* y1=b+S*D; u16* y2=y1+S*D; u16* ao=y2+S*D;
  gemm_naive<false><<<dim3(3*D/64,S/4),256,0,stream>>>(x,wqkv,qkv,S,3*D,D,3*D,3*D);
  gemm_naive<true><<<dim3(D/64,S/4),256,0,stream>>>(x,hre,a,S,D,D,D,D);
  gemm_naive<true><<<dim3(D/64,S/4),256,0,stream>>>(x,him,b,S,D,D,D,D);
  attn_naive<<<dim3(S/64,H),64,0,stream>>>(qkv,o);
  gemm_naive<false><<<dim3(D/64,S/4),256,0,stream>>>(o,wo,ao,S,D,D,D,D);
  gemm_naive<false><<<dim3(D/64,S/4),256,0,stream>>>(cre,a,y1,S,D,S,D,D);
  gemm_naive<false><<<dim3(D/64,S/4),256,0,stream>>>(cim,b,y2,S,D,S,D,D);
  mix_naive<<<S,64,0,stream>>>(x,gw,ao,y1,y2,(u16*)d_out);
}
```

```cpp
#include <hip/hip_runtime.h>
#include <hip/hip_bf16.h>

typedef unsigned short u16;
using bf16x8 = __attribute__((ext_vector_type(8))) short;
using s16x4  = __attribute__((ext_vector_type(4))) short;
using f32x16 = __attribute__((ext_vector_type(16))) float;
using f32x4  = __attribute__((ext_vector_type(4))) float;
using u32x4  = __attribute__((ext_vector_type(4))) unsigned;
typedef __bf16 bf2_t __attribute__((ext_vector_type(2)));
typedef float f2_t __attribute__((ext_vector_type(2)));
#define LAS __attribute__((address_space(3)))
#define DI __device__ __forceinline__
#define MFMA32(a, b, c) __builtin_amdgcn_mfma_f32_32x32x16_bf16(a, b, c, 0, 0, 0)
#define MFMA16(a, b, c) __builtin_amdgcn_mfma_f32_16x16x32_bf16(a, b, c, 0, 0, 0)
#define SB() __builtin_amdgcn_sched_barrier(0)
#define BAR() asm volatile("s_barrier" ::: "memory")
#define WAIT_L0() asm volatile("s_waitcnt lgkmcnt(0)" ::: "memory")
template <int N> DI void wait_v() { asm volatile("s_waitcnt vmcnt(%0)" ::"n"(N) : "memory"); }
typedef __amdgpu_buffer_rsrc_t rsrc_t;
DI rsrc_t mk_rsrc(const void* p, unsigned bytes) { return __builtin_amdgcn_make_buffer_rsrc((void*)p, 0, bytes, 0x00020000); }
DI void glds(rsrc_t r, LAS char* dst, unsigned voff, unsigned soff) {
    __builtin_amdgcn_raw_ptr_buffer_load_lds(r, (LAS void*)dst, 16, voff, soff, 0, 0);
}
DI unsigned cvtpk(float lo, float hi) { f2_t v = {lo, hi}; bf2_t r = __builtin_convertvector(v, bf2_t); return __builtin_bit_cast(unsigned, r); }
DI u32x4 pack8(const f32x4& a, const f32x4& b) { u32x4 v; v[0] = cvtpk(a[0], a[1]); v[1] = cvtpk(a[2], a[3]); v[2] = cvtpk(b[0], b[1]); v[3] = cvtpk(b[2], b[3]); return v; }
DI float bflo(unsigned u) { return __uint_as_float(u << 16); }
DI float bfhi(unsigned u) { return __uint_as_float(u & 0xffff0000u); }
DI float bfround(float f) { return bflo(cvtpk(f, f)); }
DI bf16x8 ldsr(LAS char* p) { return *(LAS bf16x8*)p; }
DI s16x4 ldtr(LAS char* p) { return __builtin_amdgcn_ds_read_tr16_b64_v4i16((LAS s16x4*)p); }
DI bf16x8 ldtr2(LAS char* p, int off2) { s16x4 a = ldtr(p), b = ldtr(p + off2); return __builtin_shufflevector(a, b, 0, 1, 2, 3, 4, 5, 6, 7); }

constexpr int SEQ = 2048, DM = 512;
constexpr size_t WS_Q = 0, WS_K = 2u << 20, WS_V = 4u << 20, WS_AB = 6u << 20, WS_O = 10u << 20, WS_P = 12u << 20, WS_WO = 20u << 20, WS_G = 21u << 20;

#define PREFETCH_C 0
#define STAMP(k, i)
#define STAMPW(k, i, t)
#define PROF_ARG

constexpr int K1_NS = 4, K1_STAGE = 36864, K1_NKT = 8;
template <bool QK>
DI void k1_body(const u16* __restrict__ x, const u16* __restrict__ wqkv, const u16* __restrict__ hmat, char* __restrict__ ws,
                LAS char* lds, int rb, int j, int lane, int wid) {
    constexpr int NW = QK ? 4 : 2;
    constexpr int NH = QK ? 1 : 3;
    constexpr int WOFF = 16384, HOFF = QK ? 32768 : 24576;
    constexpr int TOFF = QK ? 1024 : 512, KSOFF = QK ? 8192 : 4096;
    const int wm = wid >> 1, wn = wid & 1, i16 = lane & 15, g = lane >> 4;
    const unsigned voffX = (lane >> 3) * 1024 + (((lane & 7) ^ (((lane >> 4) & 1) | (wid << 1))) << 4);
    unsigned voffW;
    if (QK) { const int r = lane >> 4, v = r | (((wid >> 1) & 1) << 2), sw = (v & 1) | ((v >> 1) << 2), c = (lane & 15) ^ sw;
              voffW = r * 3072 + (c >> 3) * 1024 + (c & 7) * 16; }
    else    { const int r = lane >> 3, u = ((r >> 1) & 1) | ((wid & 1) << 1), c = (lane & 7) ^ (u << 1);
              voffW = r * 3072 + c * 16; }
    const unsigned soffX = (unsigned)(rb * 128 + wid * 8) * 1024;
    const unsigned soffW = (unsigned)(wid * (QK ? 4 : 8)) * 3072 + (QK ? 0 : 2048) + j * 128;
    const unsigned soffH = (unsigned)((j & 3) * 128 + (QK ? 0 : 32) + wid * 8) * 1024;
    auto piece = [&](int p, int kt, unsigned bufoff) {
        const bool ok = kt < K1_NKT;
        LAS char* b = lds + bufoff + wid * 1024;
        if (p < 4)           glds(mk_rsrc(x, ok ? SEQ * DM * 2 : 0), b + p * 4096, voffX, soffX + p * 32768 + kt * 128);
        else if (p < 4 + NW) glds(mk_rsrc(wqkv, ok ? DM * 3 * DM * 2 : 0), b + WOFF + (p - 4) * 4096, voffW, soffW + ((p - 4) * (QK ? 16 : 32) + kt * 64) * 3072);
        else                 glds(mk_rsrc(hmat, ok ? DM * DM * 2 : 0), b + HOFF + (p - 4 - NW) * 4096, voffX, soffH + (p - 4 - NW) * 32768 + kt * 128);
    };
    unsigned ax[2];
#pragma unroll
    for (int ks = 0; ks < 2; ++ks) ax[ks] = (8 * (i16 >> 2) + (i16 & 3)) * 128 + (((4 * ks + g) ^ (i16 >> 1)) << 4);
    unsigned aw[NW];
    { const int q = i16 >> 2, p = lane & 3;
      if (QK) { const int v = q | ((g & 1) << 2), sw = (v & 1) | ((v >> 1) << 2);
#pragma unroll
          for (int f = 0; f < NW; ++f) aw[f] = WOFF + (8 * g + q) * 256 + (((8 * wn + 4 * (f >> 1) + (f & 1) + 2 * (p >> 1)) ^ sw) << 4) + 8 * (p & 1);
      } else { const int u = (q >> 1) | ((g & 1) << 1);
#pragma unroll
          for (int f = 0; f < NW; ++f) aw[f] = WOFF + (8 * g + q) * 128 + (((4 * wn + 2 * f + (p >> 1)) ^ (u << 1)) << 4) + 8 * (p & 1);
      } }
    const unsigned xoff = wm * 8192, hoff = HOFF + wn * 512;
    f32x4 accw[4][NW], acch[4][NH];
#pragma unroll
    for (int m = 0; m < 4; ++m) {
#pragma unroll
        for (int f = 0; f < NW; ++f) accw[m][f] = f32x4{0.f, 0.f, 0.f, 0.f};
#pragma unroll
        for (int f = 0; f < NH; ++f) acch[m][f] = f32x4{0.f, 0.f, 0.f, 0.f};
    }
    bf16x8 fx[2][2], fw[2][NW], fh[2][NH];
    auto RDX = [&](int slot, unsigned buf, int ks, int half) {
        LAS char* p = lds + buf + ax[ks] + xoff + half * 4096; fx[slot][0] = ldsr(p); fx[slot][1] = ldsr(p + 512); };
    auto RDB = [&](int slot, unsigned buf, int ks, int part) {
#pragma unroll
        for (int f = 0; f < 5; ++f) if ((f < 3) == (part == 0)) {
            if (f < NW) fw[slot][f < NW ? f : 0] = ldtr2(lds + buf + aw[f < NW ? f : 0] + ks * KSOFF, TOFF);
            else fh[slot][f >= NW ? f - NW : 0] = ldsr(lds + buf + ax[ks] + hoff + (f - NW) * 4096);
        } };
    auto MM = [&](int sx, int sb, int half, int p0, int np, int kt2, unsigned buf2) {
#pragma unroll
        for (int mm = 0; mm < 2; ++mm) {
            const int m = 2 * half + mm;
#pragma unroll
            for (int f = 0; f < 5; ++f) {
                if (f < NW) { const int ff = f < NW ? f : 0;
                    if (QK) accw[m][ff] = MFMA16(fw[sb][ff], fx[sx][mm], accw[m][ff]); else accw[m][ff] = MFMA16(fx[sx][mm], fw[sb][ff], accw[m][ff]); }
                else { const int ff = f >= NW ? f - NW : 0; acch[m][ff] = MFMA16(fx[sx][mm], fh[sb][ff], acch[m][ff]); }
                const int n = mm * 5 + f;
                if (n == 1 || n == 4 || n == 7) { const int pi = (n - 1) / 3; if (pi < np) { SB(); piece(p0 + pi, kt2, buf2); SB(); } }
            }
        }
        SB(); };
#pragma unroll
    for (int s = 0; s < K1_NS - 1; ++s)
#pragma unroll
        for (int p = 0; p < 9; ++p) piece(p, s, s * K1_STAGE);
    piece(0, K1_NS - 1, (K1_NS - 1) * K1_STAGE); piece(1, K1_NS - 1, (K1_NS - 1) * K1_STAGE);
    wait_v<(K1_NS - 2) * 9 + 2>(); BAR(); SB();
    RDB(0, 0, 0, 0); RDX(0, 0, 0, 0); RDB(0, 0, 0, 1); SB();
    unsigned buf = 0, pbuf = (K1_NS - 1) * K1_STAGE;
#pragma unroll 1
    for (int kt = 0; kt < K1_NKT; ++kt) {
        unsigned nbuf = buf + K1_STAGE; if (nbuf == K1_NS * K1_STAGE) nbuf = 0;
        RDX(1, buf, 0, 1); RDB(1, buf, 1, 0); SB(); MM(0, 0, 0, 2, 3, kt + K1_NS - 1, pbuf);
        RDX(0, buf, 1, 0); RDB(1, buf, 1, 1); SB(); MM(1, 0, 1, 5, 2, kt + K1_NS - 1, pbuf);
        RDX(1, buf, 1, 1); SB();                    MM(0, 1, 0, 7, 2, kt + K1_NS - 1, pbuf);
        wait_v<(K1_NS - 2) * 9>(); WAIT_L0(); BAR(); SB();
        RDB(0, nbuf, 0, 0); RDX(0, nbuf, 0, 0); RDB(0, nbuf, 0, 1); SB();
        MM(1, 1, 1, 0, 2, kt + K1_NS, buf);
        pbuf = buf; buf = nbuf;
    }
    const int tile0 = rb * 4 + wm * 2;
    if (QK) {
        char* dst = ws + (wn ? WS_K : WS_Q) + (size_t)(4 * j + (g >> 1)) * 65536 + tile0 * 1024 + (g & 1) * 512 + ((i16 & 7) + 16 * (i16 >> 3)) * 16;
#pragma unroll
        for (int pr = 0; pr < 2; ++pr)
#pragma unroll
            for (int m = 0; m < 4; ++m)
                *(u32x4*)(dst + pr * 131072 + (m >> 1) * 1024 + (m & 1) * 128) = pack8(accw[m][(2 * pr) % NW], accw[m][(2 * pr + 1) % NW]);
    } else {
        char* dst = ws + WS_V + (size_t)(4 * j + 2 * wn) * 65536 + tile0 * 1024 + lane * 16;
#pragma unroll
        for (int f = 0; f < NW; ++f)
#pragma unroll
            for (int b = 0; b < 2; ++b) *(u32x4*)(dst + f * 65536 + b * 1024) = pack8(accw[2 * b][f], accw[2 * b + 1][f]);
    }
    {
        char* dst = ws + WS_AB + (size_t)((j >> 2) * 64 + tile0) * 32768 + ((j & 3) * 8 + (QK ? 0 : 2) + wn) * 1024 + lane * 16;
#pragma unroll
        for (int f = 0; f < NH; ++f)
#pragma unroll
            for (int b = 0; b < 2; ++b) *(u32x4*)(dst + b * 32768 + f * 2048) = pack8(acch[2 * b][f], acch[2 * b + 1][f]);
    }
}

DI void k1_helper(const u16* __restrict__ x, const u16* __restrict__ gw, const u16* __restrict__ wo, const char* __restrict__ cslice,
                  char* __restrict__ ws, int lane) {
    u32x4 gx[8], gwv; unsigned wv[16], pf[8];
    const int g = lane >> 4, i16 = lane & 15;
    const u16* xr = x + (size_t)blockIdx.x * 8 * DM + lane * 8;
#pragma unroll
    for (int r = 0; r < 8; ++r) asm volatile("global_load_dwordx4 %0, %1, off" : "=v"(gx[r]) : "v"(xr + r * DM));
    asm volatile("global_load_dwordx4 %0, %1, off" : "=v"(gwv) : "v"(gw + lane * 8));
    const int kb = blockIdx.x >> 4, eb = blockIdx.x & 15;
    const u16* wsrc = wo + (size_t)(kb * 32 + (g >> 1) * 16 + (g & 1) * 4) * DM + eb * 32 + 8 * (i16 >> 2) + (i16 & 3);
#pragma unroll
    for (int e = 0; e < 16; ++e) asm volatile("global_load_ushort %0, %1, off" : "=v"(wv[e]) : "v"(wsrc + ((e & 3) + 8 * ((e >> 2) & 1)) * DM + 4 * (e >> 3)));
#pragma unroll
    for (int r = 0; r < 8; ++r) pf[r] = 0;
#pragma unroll 1
    for (int b = 0; b < K1_NKT; ++b) BAR();
    asm volatile("s_waitcnt vmcnt(%c25)"
                 : "+v"(gx[0]), "+v"(gx[1]), "+v"(gx[2]), "+v"(gx[3]), "+v"(gx[4]), "+v"(gx[5]), "+v"(gx[6]), "+v"(gx[7]), "+v"(gwv),
                   "+v"(wv[0]), "+v"(wv[1]), "+v"(wv[2]), "+v"(wv[3]), "+v"(wv[4]), "+v"(wv[5]), "+v"(wv[6]), "+v"(wv[7]),
                   "+v"(wv[8]), "+v"(wv[9]), "+v"(wv[10]), "+v"(wv[11]), "+v"(wv[12]), "+v"(wv[13]), "+v"(wv[14]), "+v"(wv[15])
                 : "i"(PREFETCH_C ? 8 : 0) : "memory");
    BAR();
    float sel = 0.f;
#pragma unroll
    for (int r = 0; r < 8; ++r) {
        float s = 0.f;
#pragma unroll
        for (int e = 0; e < 4; ++e) s += bfround(bflo(gx[r][e]) * bflo(gwv[e])) + bfround(bfhi(gx[r][e]) * bfhi(gwv[e]));
#pragma unroll
        for (int o = 32; o > 0; o >>= 1) s += __shfl_xor(s, o);
        sel = (lane == r) ? s : sel;
    }
    const float gt = bfround(1.f / (1.f + __expf(-bfround(sel))));
    if (lane < 8) ((float*)(ws + WS_G))[blockIdx.x * 8 + lane] = gt;
#pragma unroll
    for (int f = 0; f < 2; ++f) {
        u32x4 v;
#pragma unroll
        for (int e = 0; e < 4; ++e) v[e] = wv[8 * f + 2 * e] | (wv[8 * f + 2 * e + 1] << 16);
        *(u32x4*)(ws + WS_WO + (size_t)(blockIdx.x * 2 + f) * 1024 + lane * 16) = v;
    }
    asm volatile("s_waitcnt vmcnt(0)" :: "v"(pf[0]), "v"(pf[1]), "v"(pf[2]), "v"(pf[3]), "v"(pf[4]), "v"(pf[5]), "v"(pf[6]), "v"(pf[7]) : "memory");
}

__global__ __launch_bounds__(320, 2) void k1_proj(const u16* __restrict__ x, const u16* __restrict__ wqkv, const u16* __restrict__ wo,
                                                  const u16* __restrict__ hre, const u16* __restrict__ him, const u16* __restrict__ cre,
                                                  const u16* __restrict__ cim, const u16* __restrict__ gw, char* __restrict__ ws PROF_ARG) {
    extern __shared__ __attribute__((aligned(16))) char smem[];
    STAMP(0, 0);
    const int tid = threadIdx.x, lane = tid & 63, wid = __builtin_amdgcn_readfirstlane(tid >> 6);
    const int xcd = blockIdx.x & 7, idx = blockIdx.x >> 3;
    const int rb = (xcd & 1) * 8 + (idx & 7), q4 = xcd >> 1, c4 = idx >> 3, j = q4 * 2 + (c4 >> 1);
    if (wid == 4) {
        k1_helper(x, gw, wo, (const char*)((q4 >> 1) ? cim : cre) + (size_t)((q4 & 1) * 1024 + idx * 32) * 4096 + (xcd & 1) * 2048, ws, lane);
        STAMPW(0, 2, 256);
    } else {
        if (c4 & 1) k1_body<false>(x, wqkv, j < 4 ? hre : him, ws, (LAS char*)smem, rb, j, lane, wid);
        else        k1_body<true>(x, wqkv, j < 4 ? hre : him, ws, (LAS char*)smem, rb, j, lane, wid);
        STAMP(0, 1);
    }
}

constexpr int K2_NS = 3, K2_STAGE = 32768, K2_NKT = 16;
constexpr int K2_LK = K2_NS * K2_STAGE, K2_LV = K2_LK + 16384, K2_L1 = K2_LV + 16384, K2_LDS = K2_L1 + 16384;

DI void k2_gemm(const u16* __restrict__ cmat, char* __restrict__ ws, LAS char* lds, int ksl, int tp, int nt, int lane, int wid) {
    const int wm = wid >> 1, wn = wid & 1, i16 = lane & 15, g = lane >> 4;
    const unsigned voffA = (lane >> 3) * 4096 + (((lane & 7) ^ (4 * (wid & 1) + (lane >> 4))) << 4);
    const unsigned voffB = lane * 16;
    const unsigned soffA = (unsigned)(tp * 128 + wid * 8) * 4096 + (ksl & 1) * 2048;
    const unsigned soffB = (unsigned)(ksl * 32 + (wid >> 1)) * 32768 + (nt * 8 + (wid & 1) * 4) * 1024;
    const char* abp = ws + WS_AB;
    auto piece = [&](int p, int kt, unsigned bufoff) {
        const bool ok = kt < K2_NKT;
        if ((p & 1) == 0) glds(mk_rsrc(cmat, ok ? SEQ * SEQ * 2 : 0), lds + bufoff + wid * 1024 + (p >> 1) * 4096, voffA, soffA + (p >> 1) * 131072 + kt * 128);
        else              glds(mk_rsrc(abp, ok ? 4u << 20 : 0), lds + bufoff + 16384 + wid * 4096 + (p >> 1) * 1024, voffB, soffB + kt * 65536 + (p >> 1) * 1024);
    };
    unsigned ac[2];
#pragma unroll
    for (int ks = 0; ks < 2; ++ks) ac[ks] = (wm * 64 + i16) * 128 + (((4 * ks + g) ^ (i16 >> 1)) << 4);
    const unsigned ab = 16384 + wn * 4096 + lane * 16;
    f32x4 acc[4][4];
#pragma unroll
    for (int m = 0; m < 4; ++m)
#pragma unroll
        for (int n = 0; n < 4; ++n) acc[m][n] = f32x4{0.f, 0.f, 0.f, 0.f};
    bf16x8 fc[2][2], fb[2][4];
    auto RDC = [&](int slot, unsigned buf, int ks, int half) { LAS char* p = lds + buf + ac[ks] + half * 4096; fc[slot][0] = ldsr(p); fc[slot][1] = ldsr(p + 2048); };
    auto RDB = [&](int slot, unsigned buf, int ks, int n0) { LAS char* p = lds + buf + ab + ks * 8192 + n0 * 1024; fb[slot][n0] = ldsr(p); fb[slot][n0 + 1] = ldsr(p + 1024); };
    auto MM = [&](int sc, int sb, int half, int p0, int kt2, unsigned buf2) {
#pragma unroll
        for (int mm = 0; mm < 2; ++mm)
#pragma unroll
            for (int n = 0; n < 4; ++n) {
                acc[2 * half + mm][n] = MFMA16(fb[sb][n], fc[sc][mm], acc[2 * half + mm][n]);
                if (n == 1) { SB(); piece(p0 + mm, kt2, buf2); SB(); }
            }
        SB(); };
#pragma unroll
    for (int s = 0; s < K2_NS - 1; ++s)
#pragma unroll
        for (int p = 0; p < 8; ++p) piece(p, s, s * K2_STAGE);
    piece(0, K2_NS - 1, (K2_NS - 1) * K2_STAGE); piece(1, K2_NS - 1, (K2_NS - 1) * K2_STAGE);
    wait_v<(K2_NS - 2) * 8 + 2>(); BAR(); SB();
    RDB(0, 0, 0, 0); RDC(0, 0, 0, 0); RDB(0, 0, 0, 2); SB();
    unsigned buf = 0, pbuf = (K2_NS - 1) * K2_STAGE;
#pragma unroll 1
    for (int kt = 0; kt < K2_NKT; ++kt) {
        unsigned nbuf = buf + K2_STAGE; if (nbuf == K2_NS * K2_STAGE) nbuf = 0;
        RDC(1, buf, 0, 1); RDB(1, buf, 1, 0); SB(); MM(0, 0, 0, 2, kt + K2_NS - 1, pbuf);
        RDC(0, buf, 1, 0); RDB(1, buf, 1, 2); SB(); MM(1, 0, 1, 4, kt + K2_NS - 1, pbuf);
        RDC(1, buf, 1, 1); SB();                    MM(0, 1, 0, 6, kt + K2_NS - 1, pbuf);
        wait_v<(K2_NS - 2) * 8>(); WAIT_L0(); BAR(); SB();
        RDB(0, nbuf, 0, 0); RDC(0, nbuf, 0, 0); RDB(0, nbuf, 0, 2); SB();
        MM(1, 1, 1, 0, kt + K2_NS, buf);
        pbuf = buf; buf = nbuf;
    }
    char* dst = ws + WS_P + (size_t)ksl * (2u << 20) + (size_t)(tp * 8 + wm * 4) * 16384 + (nt * 4 + wn * 2) * 1024 + lane * 16;
#pragma unroll
    for (int m = 0; m < 4; ++m)
#pragma unroll
        for (int b = 0; b < 2; ++b) *(u32x4*)(dst + m * 16384 + b * 1024) = pack8(acc[m][2 * b], acc[m][2 * b + 1]);
}

#define MX3(a, b, c) __builtin_fmaxf(__builtin_fmaxf((a), (b)), (c))
DI void k2_attn(char* __restrict__ ws, LAS char* lds, int head, int qb, int lane, int aw) {
    const int i32 = lane & 31, h = lane >> 5;
    {
        const unsigned vo = lane * 16;
#pragma unroll
        for (int i = 0; i < 4; ++i) {
            const int t = aw + 4 * i; const int gt = qb * 8 - 8 + t; const bool ok = gt >= 0;
            const unsigned so = (unsigned)head * 65536 + (ok ? gt : 0) * 1024;
            glds(mk_rsrc(ws + WS_K, ok ? 2u << 20 : 0), lds + K2_LK + t * 1024, vo, so);
            glds(mk_rsrc(ws + WS_V, ok ? 2u << 20 : 0), lds + K2_LV + t * 1024, vo, so);
        }
    }
    const int qt0 = qb * 8 + aw * 2;
    const bf16x8 q0 = *(const bf16x8*)(ws + WS_Q + (size_t)head * 65536 + qt0 * 1024 + lane * 16);
    const bf16x8 q1 = *(const bf16x8*)(ws + WS_Q + (size_t)head * 65536 + qt0 * 1024 + 1024 + lane * 16);
    if (aw == 0) {
        const u32x4 ones = {0x3f803f80u, 0x3f803f80u, 0x3f803f80u, 0x3f803f80u};
        *(LAS u32x4*)(lds + K2_L1 + i32 * 512 + h * 64) = ones;
    }
    BAR();
    wait_v<0>(); WAIT_L0(); BAR();
    const int ql = (i32 & 19) | ((i32 & 4) << 1) | ((i32 & 8) >> 1);
    const int thr = ql - 8 * h;
    const unsigned kaddr = K2_LK + aw * 2048 + lane * 16;
    const unsigned vaddr = aw * 2048 + (i32 < 16 ? K2_LV + h * 256 + i32 * 16 : K2_L1 + ((i32 >= 20 && i32 < 28) ? 64 : 0));
    constexpr float C2 = 0.25f * 1.4426950408889634f;
    const float NINF = -__builtin_inff();
    f32x16 zero16;
#pragma unroll
    for (int r = 0; r < 16; ++r) zero16[r] = 0.f;
#pragma unroll 1
    for (int it = 0; it < 2; ++it) {
        const bf16x8 qf = it ? q1 : q0;
        const int qt = qt0 + it;
        LAS char* kp = lds + kaddr + it * 1024;
        LAS char* vp = lds + vaddr + it * 1024;
        f32x16 s[8];
        {
            f32x16 s8 = MFMA32(ldsr(kp + 8 * 1024), qf, zero16);
#pragma unroll
            for (int t = 0; t < 8; ++t) s[t] = MFMA32(ldsr(kp + t * 1024), qf, zero16);
            if (qt < 8) {
#pragma unroll
                for (int t = 0; t < 8; ++t) if (qt + t < 8) {
#pragma unroll
                    for (int r = 0; r < 16; ++r) s[t][r] = NINF; }
            }
#pragma unroll
            for (int r = 0; r < 16; ++r) s[0][r] = (16 * (r >> 3) + (r & 7) > thr) ? s[0][r] : s8[r];
        }
        BAR();
        float m;
        {
            float ma = MX3(s[0][0], s[0][1], s[0][2]), mb = s[0][3];
#pragma unroll
            for (int e = 4; e < 128; e += 4) {
                ma = MX3(ma, s[e >> 4][e & 15], s[e >> 4][(e & 15) + 1]); mb = MX3(mb, s[e >> 4][(e & 15) + 2], s[e >> 4][(e & 15) + 3]); }
            m = __builtin_fmaxf(ma, mb);
            auto rr = __builtin_amdgcn_permlane32_swap(__float_as_uint(m), __float_as_uint(m), false, false);
            m = __builtin_fmaxf(__uint_as_float(rr[0]), __uint_as_float(rr[1]));
        }
        const float nm = -m * C2;
        BAR();
        f32x16 o = zero16;
#pragma unroll
        for (int t = 0; t < 8; ++t) {
            u32x4 p[2];
#pragma unroll
            for (int r = 0; r < 16; r += 2) {
                const float e0 = __builtin_amdgcn_exp2f(__builtin_fmaf(s[t][r], C2, nm)), e1 = __builtin_amdgcn_exp2f(__builtin_fmaf(s[t][r + 1], C2, nm));
                p[r >> 3][(r >> 1) & 3] = cvtpk(e0, e1);
            }
            if (t == 0) {
                u32x4 p8[2];
#pragma unroll
                for (int r = 0; r < 16; r += 2) {
                    const int k0 = 16 * (r >> 3) + (r & 7);
                    const unsigned mk = (k0 > thr ? 0xffffu : 0u) | (k0 + 1 > thr ? 0xffff0000u : 0u);
                    const unsigned v = p[r >> 3][(r >> 1) & 3];
                    p[r >> 3][(r >> 1) & 3] = v & mk; p8[r >> 3][(r >> 1) & 3] = v & ~mk;
                }
                o = MFMA32(ldsr(vp + 8 * 1024), __builtin_bit_cast(bf16x8, p8[0]), o);
                o = MFMA32(ldsr(vp + 8 * 1024 + 512), __builtin_bit_cast(bf16x8, p8[1]), o);
            }
            o = MFMA32(ldsr(vp + t * 1024), __builtin_bit_cast(bf16x8, p[0]), o);
            o = MFMA32(ldsr(vp + t * 1024 + 512), __builtin_bit_cast(bf16x8, p[1]), o);
            if (t == 0 || t == 2 || t == 4 || t == 6) { SB(); BAR(); SB(); }
        }
        const float rl = __builtin_amdgcn_rcpf(o[8]);
        u32x4 ov; ov[0] = cvtpk(o[0] * rl, o[1] * rl); ov[1] = cvtpk(o[2] * rl, o[3] * rl); ov[2] = cvtpk(o[4] * rl, o[5] * rl); ov[3] = cvtpk(o[6] * rl, o[7] * rl);
        *(u32x4*)(ws + WS_O + (size_t)(qt * 2 + (ql >> 4)) * 16384 + (head >> 1) * 1024 + ((head & 1) * 2 + h) * 256 + (ql & 15) * 16) = ov;
        if (it == 0) BAR();
    }
}

__global__ __launch_bounds__(512, 2) void k2_mix(const u16* __restrict__ cre, const u16* __restrict__ cim, char* __restrict__ ws PROF_ARG) {
    extern __shared__ __attribute__((aligned(16))) char smem[];
    STAMP(1, 0);
    const int tid = threadIdx.x, lane = tid & 63, wid = __builtin_amdgcn_readfirstlane(tid >> 6);
    const int xcd = blockIdx.x & 7, idx = blockIdx.x >> 3, q4 = xcd >> 1, rh = xcd & 1;
    if (wid < 4) {
        k2_gemm((q4 >> 1) ? cim : cre, ws, (LAS char*)smem, (q4 >> 1) * 2 + rh, (q4 & 1) * 8 + (idx >> 2), idx & 3, lane, wid);
        STAMP(1, 1);
    } else {
        k2_attn(ws, (LAS char*)smem, q4 * 8 + (idx >> 2), rh * 4 + (idx & 3), lane, wid - 4);
        STAMPW(1, 2, 256);
    }
}

constexpr int K3_LDS = 131072;
__global__ __launch_bounds__(256, 2) void k3_out(const char* __restrict__ ws, u16* __restrict__ out PROF_ARG) {
    extern __shared__ __attribute__((aligned(16))) char smem[];
    STAMP(2, 0);
    LAS char* lds = (LAS char*)smem;
    const int tid = threadIdx.x, lane = tid & 63, wid = __builtin_amdgcn_readfirstlane(tid >> 6);
    const int xcd = blockIdx.x & 7, idx = blockIdx.x >> 3;
    const int tb = xcd * 4 + (idx >> 3), eb = idx & 7;
    const int wt = wid >> 1, we = wid & 1, i16 = lane & 15, g = lane >> 4;
    const rsrc_t ro = mk_rsrc(ws + WS_O, 2u << 20), rw = mk_rsrc(ws + WS_WO, 512u << 10);
    const unsigned vo = lane * 16, so = (unsigned)(tb * 4 + wid) * 16384, sw = (unsigned)(eb * 4 + wid) * 1024;
#pragma unroll
    for (int kb = 0; kb < 16; ++kb) {
        glds(ro, lds + kb * 8192 + wid * 1024, vo, so + kb * 1024);
        glds(rw, lds + kb * 8192 + 4096 + wid * 1024, vo, sw + kb * 32768);
    }
    u32x4 pp[4][2]; float gg[2];
#pragma unroll
    for (int m = 0; m < 2; ++m) {
        const char* src = ws + WS_P + (size_t)(tb * 4 + wt * 2 + m) * 16384 + (eb * 2 + we) * 1024 + lane * 16;
#pragma unroll
        for (int k = 0; k < 4; ++k) asm volatile("global_load_dwordx4 %0, %1, off" : "=v"(pp[k][m]) : "v"(src + (size_t)k * (2u << 20)));
        asm volatile("global_load_dword %0, %1, off" : "=v"(gg[m]) : "v"((const float*)(ws + WS_G) + tb * 64 + wt * 32 + m * 16 + i16));
    }
    f32x4 acc[2][2];
#pragma unroll
    for (int m = 0; m < 2; ++m) { acc[m][0] = f32x4{0.f, 0.f, 0.f, 0.f}; acc[m][1] = f32x4{0.f, 0.f, 0.f, 0.f}; }
    LAS char* pa = lds + wt * 2048 + lane * 16;
    LAS char* pb = lds + 4096 + we * 2048 + lane * 16;
#pragma unroll
    for (int ph = 0; ph < 4; ++ph) {
        if (ph == 0) wait_v<34>(); else if (ph == 1) wait_v<26>(); else if (ph == 2) wait_v<18>(); else wait_v<10>();
        BAR(); SB();
#pragma unroll
        for (int kk = 0; kk < 4; ++kk) {
            const int kb = ph * 4 + kk;
            const bf16x8 o0 = ldsr(pa + kb * 8192), o1 = ldsr(pa + kb * 8192 + 1024), w0 = ldsr(pb + kb * 8192), w1 = ldsr(pb + kb * 8192 + 1024);
            acc[0][0] = MFMA16(w0, o0, acc[0][0]); acc[0][1] = MFMA16(w1, o0, acc[0][1]);
            acc[1][0] = MFMA16(w0, o1, acc[1][0]); acc[1][1] = MFMA16(w1, o1, acc[1][1]);
        }
        SB();
    }
    asm volatile("s_waitcnt vmcnt(0)" : "+v"(pp[0][0]), "+v"(pp[1][0]), "+v"(pp[2][0]), "+v"(pp[3][0]), "+v"(pp[0][1]), "+v"(pp[1][1]), "+v"(pp[2][1]), "+v"(pp[3][1]),
                 "+v"(gg[0]), "+v"(gg[1]) :: "memory");
#pragma unroll
    for (int m = 0; m < 2; ++m) {
        const float gt = gg[m], og = bfround(1.f - gt);
        float r[8];
#pragma unroll
        for (int e = 0; e < 8; ++e) {
            const int w = e >> 1;
            const float y1 = (e & 1) ? bfhi(pp[0][m][w]) + bfhi(pp[1][m][w]) : bflo(pp[0][m][w]) + bflo(pp[1][m][w]);
            const float y2 = (e & 1) ? bfhi(pp[2][m][w]) + bfhi(pp[3][m][w]) : bflo(pp[2][m][w]) + bflo(pp[3][m][w]);
            r[e] = gt * acc[m][e >> 2][e & 3] + og * (y1 - y2);
        }
        u32x4 v; v[0] = cvtpk(r[0], r[1]); v[1] = cvtpk(r[2], r[3]); v[2] = cvtpk(r[4], r[5]); v[3] = cvtpk(r[6], r[7]);
        *(u32x4*)(out + (size_t)(tb * 64 + wt * 32 + m * 16 + i16) * DM + eb * 64 + we * 32 + g * 8) = v;
    }
    STAMP(2, 1);
}

#define PROF_PASS
extern "C" void kernel_launch(void* const* d_in, const int* in_sizes, int n_in, void* d_out, int out_size, void* d_ws, size_t ws_size,
                              hipStream_t stream) {
    const u16* x = (const u16*)d_in[0]; const u16* wqkv = (const u16*)d_in[1]; const u16* wo = (const u16*)d_in[2];
    const u16* cre = (const u16*)d_in[4]; const u16* cim = (const u16*)d_in[5];
    const u16* hre = (const u16*)d_in[6]; const u16* him = (const u16*)d_in[7]; const u16* gw = (const u16*)d_in[8];
    char* ws = (char*)d_ws;
    (void)hipFuncSetAttribute((const void*)k1_proj, hipFuncAttributeMaxDynamicSharedMemorySize, K1_NS * K1_STAGE);
    (void)hipFuncSetAttribute((const void*)k2_mix, hipFuncAttributeMaxDynamicSharedMemorySize, K2_LDS);
    (void)hipFuncSetAttribute((const void*)k3_out, hipFuncAttributeMaxDynamicSharedMemorySize, K3_LDS);
    k1_proj<<<256, 320, K1_NS * K1_STAGE, stream>>>(x, wqkv, wo, hre, him, cre, cim, gw, ws PROF_PASS);
    k2_mix<<<256, 512, K2_LDS, stream>>>(cre, cim, ws PROF_PASS);
    k3_out<<<256, 256, K3_LDS, stream>>>(ws, (u16*)d_out PROF_PASS);
}
```

```cpp
#define PREFETCH_C 0
#include <hip/hip_runtime.h>
#include <hip/hip_bf16.h>

#define ABL 0
typedef unsigned short u16;
using bf16x8 = __attribute__((ext_vector_type(8))) short;
using s16x4  = __attribute__((ext_vector_type(4))) short;
using f32x16 = __attribute__((ext_vector_type(16))) float;
using f32x4  = __attribute__((ext_vector_type(4))) float;
using u32x4  = __attribute__((ext_vector_type(4))) unsigned;
typedef __bf16 bf2_t __attribute__((ext_vector_type(2)));
typedef float f2_t __attribute__((ext_vector_type(2)));
#define LAS __attribute__((address_space(3)))
#define DI __device__ __forceinline__
#define MFMA32(a, b, c) __builtin_amdgcn_mfma_f32_32x32x16_bf16(a, b, c, 0, 0, 0)
#define MFMA16(a, b, c) __builtin_amdgcn_mfma_f32_16x16x32_bf16(a, b, c, 0, 0, 0)
#define SB() __builtin_amdgcn_sched_barrier(0)
#define BAR() asm volatile("s_barrier" ::: "memory")
#define PIN(x) asm volatile("" : "+v"(x))
#define LGKM0() __builtin_amdgcn_s_waitcnt(0xC07F)
#define WAIT_L0() asm volatile("s_waitcnt lgkmcnt(0)" ::: "memory")
template <int N> DI void wait_v() { asm volatile("s_waitcnt vmcnt(%0)" ::"n"(N) : "memory"); }
typedef __amdgpu_buffer_rsrc_t rsrc_t;
DI rsrc_t mk_rsrc(const void* p, unsigned bytes) { return __builtin_amdgcn_make_buffer_rsrc((void*)p, 0, bytes, 0x00020000); }
template <int AUX = 0> DI void glds(rsrc_t r, LAS char* dst, unsigned voff, unsigned soff) {
    __builtin_amdgcn_raw_ptr_buffer_load_lds(r, (LAS void*)dst, 16, voff, soff, 0, AUX);
}
#define AUX_C 0
#define AUX_PF 0
#define AUX_IN 0
#define WS_END 0
DI unsigned cvtpk(float lo, float hi) { f2_t v = {lo, hi}; bf2_t r = __builtin_convertvector(v, bf2_t); return __builtin_bit_cast(unsigned, r); }
DI u32x4 pack8(const f32x4& a, const f32x4& b) { u32x4 v; v[0] = cvtpk(a[0], a[1]); v[1] = cvtpk(a[2], a[3]); v[2] = cvtpk(b[0], b[1]); v[3] = cvtpk(b[2], b[3]); return v; }
DI float bflo(unsigned u) { return __uint_as_float(u << 16); }
DI float bfhi(unsigned u) { return __uint_as_float(u & 0xffff0000u); }
DI float bfround(float f) { return bflo(cvtpk(f, f)); }
DI LAS char* LP(unsigned a) { return (LAS char*)(size_t)a; }
DI unsigned LA(LAS char* p) { return (unsigned)(size_t)p; }
DI bf16x8 ldsr(LAS char* p) { return *(LAS bf16x8*)p; }
DI s16x4 ldtr(LAS char* p) { return __builtin_amdgcn_ds_read_tr16_b64_v4i16((LAS s16x4*)p); }
DI bf16x8 ldtr2(LAS char* p, int off2) { s16x4 a = ldtr(p), b = ldtr(p + off2); return __builtin_shufflevector(a, b, 0, 1, 2, 3, 4, 5, 6, 7); }

#define ST_MODE 0
DI void ST16(void* p, u32x4 v) {
    *(u32x4*)p = v;
}
constexpr int SEQ = 2048, DM = 512;
constexpr size_t WS_Q = 0, WS_K = 2u << 20, WS_V = 4u << 20, WS_AB = 6u << 20, WS_O = 10u << 20, WS_P = 12u << 20, WS_WO = 20u << 20, WS_G = 21u << 20;

#define STAMP(k, i)
#define STAMPW(k, i, t)
#define PROF_ARG
#define PROF_FWD
#define RT(v)
#define CT(v)
#define PUT(k, i, v)
#define PUTC(k, i, v)

constexpr int K1_NS = 4, K1_NKT = 8, K1_DUMMY = 147456, K1_LDS = K1_DUMMY + 1024;
#define PF_A -1
#define PF_B 3
template <bool QK>
DI void k1_load(const u16* __restrict__ x, const u16* __restrict__ wqkv, const u16* __restrict__ hmat, const u16* __restrict__ cmat, unsigned coff,
                const u16* __restrict__ gw, const u16* __restrict__ wo, char* __restrict__ ws, LAS char* lds, int rb, int j, int lane, int wid) {
    constexpr int NW = QK ? 4 : 2;
    constexpr int WOFF = 65536, WST = NW * 4096, HOFF = WOFF + 4 * WST, HST = (5 - NW) * 4096;
    const unsigned voffX = (lane >> 3) * 1024 + (((lane & 7) ^ (((lane >> 4) & 1) | (wid << 1))) << 4);
    unsigned voffW;
    if (QK) { const int r = lane >> 4, v = r | (((wid >> 1) & 1) << 2), sw = (v & 1) | ((v >> 1) << 2), c = (lane & 15) ^ sw;
              voffW = r * 3072 + (c >> 3) * 1024 + (c & 7) * 16; }
    else    { const int r = lane >> 3, u = ((r >> 1) & 1) | ((wid & 1) << 1), c = (lane & 7) ^ (u << 1);
              voffW = r * 3072 + c * 16; }
    const unsigned soffX = (unsigned)(rb * 128 + wid * 8) * 1024;
    const unsigned soffW = (unsigned)(wid * (QK ? 4 : 8)) * 3072 + (QK ? 0 : 2048) + j * 128;
    const unsigned soffH = (unsigned)((j & 3) * 128 + (QK ? 0 : 32) + wid * 8) * 1024;
    const rsrc_t rx = mk_rsrc(x, SEQ * DM * 2), rw = mk_rsrc(wqkv, DM * 3 * DM * 2), rh = mk_rsrc(hmat, DM * DM * 2), rc = mk_rsrc(cmat, SEQ * SEQ * 2);
    auto stage = [&](int kt) {
        LAS char* b = lds + wid * 1024; const int sl = kt % K1_NS;
#pragma unroll
        for (int p = 0; p < 9; ++p) {
            if (p < 4)           glds<AUX_IN>(rx, b + sl * 16384 + p * 4096, voffX, soffX + p * 32768 + kt * 128);
            else if (p < 4 + NW) glds<AUX_IN>(rw, b + WOFF + sl * WST + (p - 4) * 4096, voffW, soffW + ((p - 4) * (QK ? 16 : 32) + kt * 64) * 3072);
            else                 glds<AUX_IN>(rh, b + HOFF + sl * HST + (p - 4 - NW) * 4096, voffX, soffH + (p - 4 - NW) * 32768 + kt * 128);
            SB();
        } };
    const unsigned voffC = (lane >> 3) * 4096 + (lane & 7) * 128;
    auto warm = [&](int hh) {
    };
    constexpr int PW = PREFETCH_C ? 1 : 0;
#pragma unroll
    for (int s = 0; s < K1_NS; ++s) stage(s);
    if (PF_A < 0) warm(0);
    if (PF_B < 0) warm(1);
    wait_v<(K1_NS - 1) * 9 + PW * ((PF_A < 0) + (PF_B < 0))>(); BAR();
    u32x4 gx0, gx1, gwv; unsigned wv[8];
#pragma unroll
    for (int kt = 0; kt < K1_NKT - 1; ++kt) {
        constexpr int dummy = 0; (void)dummy;
        const int later = (kt + K1_NS - 1 < K1_NKT ? kt + K1_NS - 1 : K1_NKT - 1) - (kt + 1);
        const int sA = PF_A + K1_NS, sB = PF_B + K1_NS;
        const int wa = (PF_A < kt && sA >= kt + 1) ? PW : 0, wb = (PF_B < kt && sB >= kt + 1) ? PW : 0;
        const int tail = (kt > K1_NKT - K1_NS - 1) ? 11 : 0;
        const int n = later * 9 + wa + wb + tail;
        asm volatile("s_waitcnt vmcnt(%0)" :: "n"(n) : "memory");
        BAR();
        if (kt + K1_NS < K1_NKT && ABL != 3) stage(kt + K1_NS);
        if (kt == K1_NKT - K1_NS - 1) {
            const u16* xr = x + (size_t)(blockIdx.x * 8 + wid * 2) * DM + lane * 8;
            asm volatile("global_load_dwordx4 %0, %1, off" : "=v"(gx0) : "v"(xr));
            asm volatile("global_load_dwordx4 %0, %1, off" : "=v"(gx1) : "v"(xr + DM));
            asm volatile("global_load_dwordx4 %0, %1, off" : "=v"(gwv) : "v"(gw + lane * 8));
            const int g = lane >> 4, i16 = lane & 15, kb = blockIdx.x >> 4, eb = blockIdx.x & 15;
            const u16* wsrc = wo + (size_t)(kb * 32 + (g >> 1) * 16 + (g & 1) * 4) * DM + eb * 32 + 8 * (i16 >> 2) + (i16 & 3) + 4 * (wid & 1);
#pragma unroll
            for (int e = 0; e < 8; ++e) asm volatile("global_load_ushort %0, %1, off" : "=v"(wv[e]) : "v"(wsrc + ((e & 3) + 8 * (e >> 2)) * DM));
        }
        if (kt == PF_A) warm(0);
        if (kt == PF_B) warm(1);
    }
    BAR();
    asm volatile("s_waitcnt vmcnt(%c11)"
                 : "+v"(gx0), "+v"(gx1), "+v"(gwv), "+v"(wv[0]), "+v"(wv[1]), "+v"(wv[2]), "+v"(wv[3]), "+v"(wv[4]), "+v"(wv[5]), "+v"(wv[6]), "+v"(wv[7])
                 : "i"(PW * ((PF_A >= K1_NKT - K1_NS - 1) + (PF_B >= K1_NKT - K1_NS - 1))) : "memory");
    float s0 = 0.f, s1 = 0.f;
#pragma unroll
    for (int e = 0; e < 4; ++e) {
        s0 += bfround(bflo(gx0[e]) * bflo(gwv[e])) + bfround(bfhi(gx0[e]) * bfhi(gwv[e]));
        s1 += bfround(bflo(gx1[e]) * bflo(gwv[e])) + bfround(bfhi(gx1[e]) * bfhi(gwv[e]));
    }
#pragma unroll
    for (int o = 32; o > 0; o >>= 1) { s0 += __shfl_xor(s0, o); s1 += __shfl_xor(s1, o); }
    const float gt = bfround(1.f / (1.f + __expf(-bfround(lane ? s1 : s0))));
    if (lane < 2) ((float*)(ws + WS_G))[blockIdx.x * 8 + wid * 2 + lane] = gt;
    if (wid < 2) {
        u32x4 v;
#pragma unroll
        for (int e = 0; e < 4; ++e) v[e] = wv[2 * e] | (wv[2 * e + 1] << 16);
        *(u32x4*)(ws + WS_WO + (size_t)(blockIdx.x * 2 + wid) * 1024 + lane * 16) = v;
    }
}

template <bool QK>
DI void k1_mma(char* __restrict__ ws, LAS char* lds, int rb, int j, int lane, int wid PROF_ARG) {
    constexpr int NW = QK ? 4 : 2;
    constexpr int NH = QK ? 1 : 3;
    constexpr int WOFF = 65536, WST = NW * 4096, HOFF = WOFF + 4 * WST, HST = NH * 4096;
    constexpr int TOFF = QK ? 1024 : 512, KSOFF = QK ? 8192 : 4096;
    const int wm = wid >> 1, wn = wid & 1, i16 = lane & 15, g = lane >> 4;
    unsigned ax[2], ah[2];
#pragma unroll
    for (int ks = 0; ks < 2; ++ks) {
        const unsigned a = (8 * (i16 >> 2) + (i16 & 3)) * 128 + (((4 * ks + g) ^ (i16 >> 1)) << 4);
        ax[ks] = LA(lds) + a + wm * 8192; ah[ks] = LA(lds) + a + HOFF + wn * 512; }
    unsigned aw[NW];
    { const int q = i16 >> 2, p = lane & 3;
      if (QK) { const int v = q | ((g & 1) << 2), sw = (v & 1) | ((v >> 1) << 2);
#pragma unroll
          for (int f = 0; f < NW; ++f) aw[f] = LA(lds) + WOFF + (8 * g + q) * 256 + (((8 * wn + 4 * (f >> 1) + (f & 1) + 2 * (p >> 1)) ^ sw) << 4) + 8 * (p & 1);
      } else { const int u = (q >> 1) | ((g & 1) << 1);
#pragma unroll
          for (int f = 0; f < NW; ++f) aw[f] = LA(lds) + WOFF + (8 * g + q) * 128 + (((4 * wn + 2 * f + (p >> 1)) ^ (u << 1)) << 4) + 8 * (p & 1);
      } }
    f32x4 accw[4][NW], acch[4][NH];
#pragma unroll
    for (int m = 0; m < 4; ++m) {
#pragma unroll
        for (int f = 0; f < NW; ++f) accw[m][f] = f32x4{0.f, 0.f, 0.f, 0.f};
#pragma unroll
        for (int f = 0; f < NH; ++f) acch[m][f] = f32x4{0.f, 0.f, 0.f, 0.f};
    }
    bf16x8 fx[2][4], fh[2][NH]; s16x4 fw[2][NW][2];
    constexpr int NRD = 4 + 2 * NW + NH;
    auto RD = [&](int n, int slot, int sl, int ks) {
        if (n == 0 || n >= NRD - 3) { const int m = n == 0 ? 0 : n - (NRD - 4); fx[slot][m] = ldsr(LP(ax[ks]) + sl * 16384 + (m >> 1) * 4096 + (m & 1) * 512); }
        else if (n - 1 < 2 * NW) { const int f = (n - 1 < 2 * NW) ? (n - 1) >> 1 : 0, t = (n - 1) & 1; fw[slot][f][t] = ldtr(LP(aw[f]) + sl * WST + ks * KSOFF + t * TOFF); }
        else { const int f = (n - 1 >= 2 * NW) ? n - 1 - 2 * NW : 0; fh[slot][f] = ldsr(LP(ah[ks]) + sl * HST + f * 4096); }
    };
    auto HALF = [&](int slot, bool rd, int nbuf, int nks) {
#pragma unroll
        for (int n = 0; n < 20; ++n) {
            const int m = n / 5, f = n % 5;
            if (f < NW) { const int ff = f < NW ? f : 0;
                const bf16x8 w8 = __builtin_shufflevector(fw[slot][ff][0], fw[slot][ff][1], 0, 1, 2, 3, 4, 5, 6, 7);
                if (QK) accw[m][ff] = MFMA16(w8, fx[slot][m], accw[m][ff]); else accw[m][ff] = MFMA16(fx[slot][m], w8, accw[m][ff]); }
            else { const int ff = f >= NW ? f - NW : 0; acch[m][ff] = MFMA16(fx[slot][m], fh[slot][ff], acch[m][ff]); }
            SB();
            if (rd && n < NRD) { RD(n, slot ^ 1, nbuf, nks); SB(); }
        }
        if (rd) { LGKM0(); SB(); } };
#pragma unroll
    for (int f = 0; f < NW; ++f) PIN(aw[f]);
    PIN(ax[0]); PIN(ax[1]); PIN(ah[0]); PIN(ah[1]);
#pragma unroll
    for (int m = 0; m < 4; ++m) {
#pragma unroll
        for (int f = 0; f < NW; ++f) PIN(accw[m][f]);
#pragma unroll
        for (int f = 0; f < NH; ++f) PIN(acch[m][f]);
    }
    RT(t3);
    BAR(); SB();
    RT(t4); CT(c4);
#pragma unroll
    for (int n = 0; n < NRD; ++n) RD(n, 0, 0, 0);
    LGKM0(); SB();
    unsigned cs = 0;
#pragma unroll 1
    for (int it = 0; it < 2; ++it) {
#pragma unroll
        for (int sl = 0; sl < 4; ++sl) {
            HALF(0, true, sl, 1);
            BAR(); SB();
            HALF(1, true, (sl + 1) & 3, 0);
        }
    }
    RT(t5); CT(c5);
    PUT(0, 3, t3); PUT(0, 4, t4); PUT(0, 5, t5);
    const int tile0 = rb * 4 + wm * 2;
    if (QK) {
        char* dst = ws + (wn ? WS_K : WS_Q) + (size_t)(4 * j + (g >> 1)) * 65536 + tile0 * 1024 + (g & 1) * 512 + ((i16 & 7) + 16 * (i16 >> 3)) * 16;
#pragma unroll
        for (int pr = 0; pr < 2; ++pr)
#pragma unroll
            for (int m = 0; m < 4; ++m)
                ST16(dst + pr * 131072 + (m >> 1) * 1024 + (m & 1) * 128, pack8(accw[m][(2 * pr) % NW], accw[m][(2 * pr + 1) % NW]));
    } else {
        char* dst = ws + WS_V + (size_t)(4 * j + 2 * wn) * 65536 + tile0 * 1024 + lane * 16;
#pragma unroll
        for (int f = 0; f < NW; ++f)
#pragma unroll
            for (int b = 0; b < 2; ++b) ST16(dst + f * 65536 + b * 1024, pack8(accw[2 * b][f], accw[2 * b + 1][f]));
    }
    {
        char* dst = ws + WS_AB + (size_t)((j >> 2) * 64 + tile0) * 32768 + ((j & 3) * 8 + (QK ? 0 : 2) + wn) * 1024 + lane * 16;
#pragma unroll
        for (int f = 0; f < NH; ++f)
#pragma unroll
            for (int b = 0; b < 2; ++b) ST16(dst + b * 32768 + f * 2048, pack8(acch[2 * b][f], acch[2 * b + 1][f]));
    }
}

__global__ __launch_bounds__(512, 2) void k1_proj(const u16* __restrict__ x, const u16* __restrict__ wqkv, const u16* __restrict__ wo,
                                                  const u16* __restrict__ hre, const u16* __restrict__ him, const u16* __restrict__ cre,
                                                  const u16* __restrict__ cim, const u16* __restrict__ gw, char* __restrict__ ws PROF_ARG) {
    extern __shared__ __attribute__((aligned(16))) char smem[];
    STAMP(0, 0);
    const int tid = threadIdx.x, lane = tid & 63, wid = __builtin_amdgcn_readfirstlane(tid >> 6);
    const int xcd = blockIdx.x & 7, idx = blockIdx.x >> 3;
    const int rb = (xcd & 1) * 8 + (idx & 7), q4 = xcd >> 1, c4 = idx >> 3, j = q4 * 2 + (c4 >> 1);
    if (wid >= 4) {
        const u16* cm = (q4 >> 1) ? cim : cre; const u16* hm = j < 4 ? hre : him;
        const unsigned coff = (unsigned)((q4 & 1) * 1024 + idx * 32) * 4096 + (xcd & 1) * 2048;
        if (c4 & 1) k1_load<false>(x, wqkv, hm, cm, coff, gw, wo, ws, (LAS char*)smem, rb, j, lane, wid - 4);
        else        k1_load<true>(x, wqkv, hm, cm, coff, gw, wo, ws, (LAS char*)smem, rb, j, lane, wid - 4);
        STAMPW(0, 2, 256);
    } else {
        if (c4 & 1) k1_mma<false>(ws, (LAS char*)smem, rb, j, lane, wid PROF_FWD);
        else        k1_mma<true>(ws, (LAS char*)smem, rb, j, lane, wid PROF_FWD);
        STAMP(0, 1);
    }
}

#define AT_PRIO 0
#define K2_PRIO 1
constexpr int K2_NS = 3, K2_BOFF = K2_NS * 16384, K2_NKT = 16;
constexpr int K2_LK = 2 * K2_BOFF, K2_LV = K2_LK + 16384, K2_L1 = K2_LV + 16384, K2_LDS = K2_L1 + 16384;

DI void k2_gemm(const u16* __restrict__ cmat, char* __restrict__ ws, LAS char* lds, int ksl, int tp, int nt, int lane, int wid PROF_ARG) {
    const int wm = wid >> 1, wn = wid & 1, i32 = lane & 31, h = lane >> 5;
    const unsigned voffA = (lane >> 3) * 4096 + (((lane & 7) ^ (4 * (wid & 1) + (lane >> 4))) << 4);
    const unsigned voffB = lane * 16;
    const unsigned soffA = (unsigned)(tp * 128 + wid * 8) * 4096 + (ksl & 1) * 2048;
    const unsigned soffB = (unsigned)(ksl * 32 + (wid >> 1)) * 32768 + (nt * 8 + (wid & 1) * 4) * 1024;
    const char* abp = ws + WS_AB;
    auto piece = [&](int p, int kt, int sl) {
        const bool ok = kt < K2_NKT;
        if ((p & 1) == 0) glds<AUX_C>(mk_rsrc(cmat, ok ? SEQ * SEQ * 2 : 0), lds + sl * 16384 + wid * 1024 + (p >> 1) * 4096, voffA, soffA + (p >> 1) * 131072 + kt * 128);
        else              glds(mk_rsrc(abp, ok ? 4u << 20 : 0), lds + K2_BOFF + sl * 16384 + wid * 4096 + (p >> 1) * 1024, voffB, soffB + kt * 65536 + (p >> 1) * 1024);
    };
    unsigned ac[4];
#pragma unroll
    for (int ks = 0; ks < 4; ++ks) ac[ks] = LA(lds) + (wm * 64 + i32) * 128 + (((2 * ks + h) ^ ((i32 >> 1) & 7)) << 4);
    unsigned ab = LA(lds) + K2_BOFF + wn * 4096 + (i32 >> 4) * 1024 + h * 256 + (i32 & 15) * 16;
    f32x16 acc[2][2];
#pragma unroll
    for (int m = 0; m < 2; ++m)
#pragma unroll
        for (int n = 0; n < 2; ++n)
#pragma unroll
            for (int r = 0; r < 16; ++r) acc[m][n][r] = 0.f;
    bf16x8 fc[2][2], fb[2][2];
    auto RDC = [&](int slot, int m, int sl, int ks) { fc[slot][m] = ldsr(LP(ac[ks]) + sl * 16384 + m * 4096); };
    auto RDB = [&](int slot, int n, int sl, int ks) { fb[slot][n] = ldsr(LP(ab) + sl * 16384 + (ks >> 1) * 8192 + (ks & 1) * 512 + n * 2048); };
    auto QUART = [&](int slot, int nbuf, int nks, int p0, int kt2, int buf2) {
        acc[0][0] = MFMA32(fb[slot][0], fc[slot][0], acc[0][0]); SB();
        RDB(slot ^ 1, 0, nbuf, nks); RDC(slot ^ 1, 0, nbuf, nks); SB();
        acc[0][1] = MFMA32(fb[slot][1], fc[slot][0], acc[0][1]); SB();
        RDB(slot ^ 1, 1, nbuf, nks); RDC(slot ^ 1, 1, nbuf, nks); SB();
        acc[1][1] = MFMA32(fb[slot][1], fc[slot][1], acc[1][1]); SB();
        if (p0 >= 0) { piece(p0, kt2, buf2); SB(); }
        acc[1][0] = MFMA32(fb[slot][0], fc[slot][1], acc[1][0]); SB();
        if (p0 >= 0) { piece(p0 + 1, kt2, buf2); SB(); }
    };
#pragma unroll
    for (int s = 0; s < K2_NS - 1; ++s)
#pragma unroll
        for (int p = 0; p < 8; ++p) piece(p, s, s);
    piece(0, K2_NS - 1, K2_NS - 1); piece(1, K2_NS - 1, K2_NS - 1);
    __builtin_amdgcn_s_setprio(K2_PRIO);
    PIN(ac[0]); PIN(ac[1]); PIN(ac[2]); PIN(ac[3]); PIN(ab);
    PIN(acc[0][0]); PIN(acc[0][1]); PIN(acc[1][0]); PIN(acc[1][1]);
    RT(t3);
    wait_v<(K2_NS - 2) * 8 + 2>(); BAR(); SB();
    RT(t4); CT(c4);
    RDB(0, 0, 0, 0); RDC(0, 0, 0, 0); RDB(0, 1, 0, 0); RDC(0, 1, 0, 0); LGKM0(); SB();
    unsigned cs = 0;
#pragma unroll 1
    for (int k0 = 0; k0 < K2_NKT - 1; k0 += 3) {
#pragma unroll
        for (int sl = 0; sl < 3; ++sl) {
            const int kt = k0 + sl;
            QUART(0, sl, 1, 2, kt + 2, (sl + 2) % 3);
            QUART(1, sl, 2, 4, kt + 2, (sl + 2) % 3);
            QUART(0, sl, 3, 6, kt + 2, (sl + 2) % 3);
            wait_v<8>(); LGKM0(); BAR(); SB();
            QUART(1, (sl + 1) % 3, 0, 0, kt + 3, sl);
        }
    }
    QUART(0, 0, 1, -1, 0, 0); QUART(1, 0, 2, -1, 0, 0); QUART(0, 0, 3, -1, 0, 0); QUART(1, 0, 0, -1, 0, 0);
    RT(t5); CT(c5);
    PUT(1, 3, t3); PUT(1, 4, t4); PUT(1, 5, t5);
    char* dst = ws + WS_P + (size_t)ksl * (2u << 20) + (size_t)(tp * 8 + wm * 4 + (i32 >> 4)) * 16384 + (nt * 4 + wn * 2) * 1024 + h * 256 + (i32 & 15) * 16;
#pragma unroll
    for (int m = 0; m < 2; ++m)
#pragma unroll
        for (int n = 0; n < 2; ++n)
#pragma unroll
            for (int b = 0; b < 2; ++b) {
                const f32x16& a = acc[m][n];
                u32x4 v; v[0] = cvtpk(a[4 * b], a[4 * b + 1]); v[1] = cvtpk(a[4 * b + 2], a[4 * b + 3]);
                v[2] = cvtpk(a[8 + 4 * b], a[9 + 4 * b]); v[3] = cvtpk(a[10 + 4 * b], a[11 + 4 * b]);
                ST16(dst + m * 32768 + n * 1024 + b * 512, v);
            }
}

#define MX3(a, b, c) __builtin_fmaxf(__builtin_fmaxf((a), (b)), (c))
#define PACE() do { SB(); BAR(); SB(); } while (0)
DI void k2_attn(char* __restrict__ ws, LAS char* lds, int head, int qb, int lane, int aw) {
    const int i32 = lane & 31, h = lane >> 5;
    {
        const unsigned vo = lane * 16;
#pragma unroll
        for (int i = 0; i < 4; ++i) {
            const int t = aw + 4 * i; const int gt = qb * 8 - 8 + t; const bool ok = gt >= 0;
            const unsigned so = (unsigned)head * 65536 + (ok ? gt : 0) * 1024;
            glds(mk_rsrc(ws + WS_K, ok ? 2u << 20 : 0), lds + K2_LK + t * 1024, vo, so);
            glds(mk_rsrc(ws + WS_V, ok ? 2u << 20 : 0), lds + K2_LV + t * 1024, vo, so);
        }
    }
    const int qt0 = qb * 8 + aw * 2;
    bf16x8 q0 = *(const bf16x8*)(ws + WS_Q + (size_t)head * 65536 + qt0 * 1024 + lane * 16);
    bf16x8 q1 = *(const bf16x8*)(ws + WS_Q + (size_t)head * 65536 + qt0 * 1024 + 1024 + lane * 16);
    if (aw == 0) {
        const u32x4 ones = {0x3f803f80u, 0x3f803f80u, 0x3f803f80u, 0x3f803f80u};
        *(LAS u32x4*)(lds + K2_L1 + i32 * 512 + h * 64) = ones;
    }
    __builtin_amdgcn_s_setprio(AT_PRIO);
    BAR();
    wait_v<0>(); WAIT_L0(); BAR();
    const int ql = (i32 & 19) | ((i32 & 4) << 1) | ((i32 & 8) >> 1);
    const int thr = ql - 8 * h;
    const unsigned kaddr = K2_LK + aw * 2048 + lane * 16;
    const unsigned vaddr = aw * 2048 + (i32 < 16 ? K2_LV + h * 256 + i32 * 16 : K2_L1 + ((i32 >= 20 && i32 < 28) ? 64 : 0));
    constexpr float C2 = 0.25f * 1.4426950408889634f;
    const float NINF = -__builtin_inff();
    f32x16 zero16;
#pragma unroll
    for (int r = 0; r < 16; ++r) zero16[r] = 0.f;
#pragma unroll 1
    for (int it = 0; it < 2; ++it) {
        const bf16x8 qf = it ? q1 : q0;
        const int qt = qt0 + it;
        LAS char* kp = lds + kaddr + it * 1024;
        LAS char* vp = lds + vaddr + it * 1024;
        f32x16 s[8];
        {
            bf16x8 kf[9];
#pragma unroll
            for (int t = 0; t < 9; ++t) kf[t] = ldsr(kp + (t == 0 ? 8 : t - 1) * 1024);
            SB();
            f32x16 s8 = MFMA32(kf[0], qf, zero16);
#pragma unroll
            for (int t = 0; t < 8; ++t) s[t] = MFMA32(kf[t + 1], qf, zero16);
            if (qt < 8) {
#pragma unroll
                for (int t = 0; t < 8; ++t) if (qt + t < 8) {
#pragma unroll
                    for (int r = 0; r < 16; ++r) s[t][r] = NINF; }
            }
#pragma unroll
            for (int r = 0; r < 16; ++r) s[0][r] = (16 * (r >> 3) + (r & 7) > thr) ? s[0][r] : s8[r];
            PIN(s[0]);
        }
        PACE();
#pragma unroll
        for (int t = 0; t < 8; ++t) PIN(s[t]);
        float m;
        {
            float ma = MX3(s[0][0], s[0][1], s[0][2]), mb = s[0][3];
#pragma unroll
            for (int e = 4; e < 128; e += 4) {
                ma = MX3(ma, s[e >> 4][e & 15], s[e >> 4][(e & 15) + 1]); mb = MX3(mb, s[e >> 4][(e & 15) + 2], s[e >> 4][(e & 15) + 3]); }
            m = __builtin_fmaxf(ma, mb);
            auto rr = __builtin_amdgcn_permlane32_swap(__float_as_uint(m), __float_as_uint(m), false, false);
            m = __builtin_fmaxf(__uint_as_float(rr[0]), __uint_as_float(rr[1]));
        }
        float nm = -m * C2;
        f32x16 o = zero16;
#pragma unroll
        for (int u = 0; u < 16; ++u) {
            const int t = u >> 1, r0 = (u & 1) * 8;
            const bf16x8 vf = ldsr(vp + t * 1024 + (u & 1) * 512);
            u32x4 p;
#pragma unroll
            for (int r = 0; r < 8; r += 2) {
                const float e0 = __builtin_amdgcn_exp2f(__builtin_fmaf(s[t][r0 + r], C2, nm)), e1 = __builtin_amdgcn_exp2f(__builtin_fmaf(s[t][r0 + r + 1], C2, nm));
                p[r >> 1] = cvtpk(e0, e1);
            }
            if (t == 0) {
                u32x4 p8;
#pragma unroll
                for (int r = 0; r < 8; r += 2) {
                    const int k0 = 2 * r0 + r;
                    const unsigned mk = (k0 > thr ? 0xffffu : 0u) | (k0 + 1 > thr ? 0xffff0000u : 0u);
                    p8[r >> 1] = p[r >> 1] & ~mk; p[r >> 1] &= mk;
                }
                o = MFMA32(ldsr(vp + 8 * 1024 + (u & 1) * 512), __builtin_bit_cast(bf16x8, p8), o);
            }
            o = MFMA32(vf, __builtin_bit_cast(bf16x8, p), o);
            if (u % 3 == 0 && u < 15) { PACE(); PIN(nm); }
        }
        PACE();
        const float rl = __builtin_amdgcn_rcpf(o[8]);
        u32x4 ov; ov[0] = cvtpk(o[0] * rl, o[1] * rl); ov[1] = cvtpk(o[2] * rl, o[3] * rl); ov[2] = cvtpk(o[4] * rl, o[5] * rl); ov[3] = cvtpk(o[6] * rl, o[7] * rl);
        ST16(ws + WS_O + (size_t)(qt * 2 + (ql >> 4)) * 16384 + (head >> 1) * 1024 + ((head & 1) * 2 + h) * 256 + (ql & 15) * 16, ov);
    }
}

__global__ __launch_bounds__(512, 2) void k2_mix(const u16* __restrict__ cre, const u16* __restrict__ cim, char* __restrict__ ws PROF_ARG) {
    extern __shared__ __attribute__((aligned(16))) char smem[];
    STAMP(1, 0);
    const int tid = threadIdx.x, lane = tid & 63, wid = __builtin_amdgcn_readfirstlane(tid >> 6);
    const int xcd = blockIdx.x & 7, idx = blockIdx.x >> 3, q4 = xcd >> 1, rh = xcd & 1;
    if (wid < 4) {
        k2_gemm((q4 >> 1) ? cim : cre, ws, (LAS char*)smem, (q4 >> 1) * 2 + rh, (q4 & 1) * 8 + (idx >> 2), idx & 3, lane, wid PROF_FWD);
        STAMP(1, 1);
    } else {
        if (ABL != 5) k2_attn(ws, (LAS char*)smem, q4 * 8 + (idx >> 2), rh * 4 + (idx & 3), lane, wid - 4);
        STAMPW(1, 2, 256);
    }
}

constexpr int K3_LDS = 131072;
__global__ __launch_bounds__(256, 2) void k3_out(const char* __restrict__ ws, u16* __restrict__ out PROF_ARG) {
    extern __shared__ __attribute__((aligned(16))) char smem[];
    STAMP(2, 0);
    LAS char* lds = (LAS char*)smem;
    const int tid = threadIdx.x, lane = tid & 63, wid = __builtin_amdgcn_readfirstlane(tid >> 6);
    const int xcd = blockIdx.x & 7, idx = blockIdx.x >> 3;
    const int tb = xcd * 4 + (idx >> 3), eb = idx & 7;
    const int wt = wid >> 1, we = wid & 1, i16 = lane & 15, g = lane >> 4;
    const rsrc_t ro = mk_rsrc(ws + WS_O, 2u << 20), rw = mk_rsrc(ws + WS_WO, 512u << 10);
    const unsigned vo = lane * 16, so = (unsigned)(tb * 4 + wid) * 16384, sw = (unsigned)(eb * 4 + wid) * 1024;
#pragma unroll
    for (int kb = 0; kb < 16; ++kb) {
        glds(ro, lds + kb * 8192 + wid * 1024, vo, so + kb * 1024);
        glds(rw, lds + kb * 8192 + 4096 + wid * 1024, vo, sw + kb * 32768);
    }
    u32x4 pp[4][2]; float gg[2];
#pragma unroll
    for (int m = 0; m < 2; ++m) {
        const char* src = ws + WS_P + (size_t)(tb * 4 + wt * 2 + m) * 16384 + (eb * 2 + we) * 1024 + lane * 16;
#pragma unroll
        for (int k = 0; k < 4; ++k) asm volatile("global_load_dwordx4 %0, %1, off" : "=v"(pp[k][m]) : "v"(src + (size_t)k * (2u << 20)));
        asm volatile("global_load_dword %0, %1, off" : "=v"(gg[m]) : "v"((const float*)(ws + WS_G) + tb * 64 + wt * 32 + m * 16 + i16));
    }
    f32x4 acc[2][2];
#pragma unroll
    for (int m = 0; m < 2; ++m) { acc[m][0] = f32x4{0.f, 0.f, 0.f, 0.f}; acc[m][1] = f32x4{0.f, 0.f, 0.f, 0.f}; }
    LAS char* pa = lds + wt * 2048 + lane * 16;
    LAS char* pb = lds + 4096 + we * 2048 + lane * 16;
#pragma unroll
    for (int ph = 0; ph < 4; ++ph) {
        if (ph == 0) wait_v<34>(); else if (ph == 1) wait_v<26>(); else if (ph == 2) wait_v<18>(); else wait_v<10>();
        BAR(); SB();
#pragma unroll
        for (int kk = 0; kk < 4; ++kk) {
            const int kb = ph * 4 + kk;
            const bf16x8 o0 = ldsr(pa + kb * 8192), o1 = ldsr(pa + kb * 8192 + 1024), w0 = ldsr(pb + kb * 8192), w1 = ldsr(pb + kb * 8192 + 1024);
            acc[0][0] = MFMA16(w0, o0, acc[0][0]); acc[0][1] = MFMA16(w1, o0, acc[0][1]);
            acc[1][0] = MFMA16(w0, o1, acc[1][0]); acc[1][1] = MFMA16(w1, o1, acc[1][1]);
        }
        SB();
    }
    asm volatile("s_waitcnt vmcnt(0)" : "+v"(pp[0][0]), "+v"(pp[1][0]), "+v"(pp[2][0]), "+v"(pp[3][0]), "+v"(pp[0][1]), "+v"(pp[1][1]), "+v"(pp[2][1]), "+v"(pp[3][1]),
                 "+v"(gg[0]), "+v"(gg[1]) :: "memory");
#pragma unroll
    for (int m = 0; m < 2; ++m) {
        const float gt = gg[m], og = bfround(1.f - gt);
        float r[8];
#pragma unroll
        for (int e = 0; e < 8; ++e) {
            const int w = e >> 1;
            const float y1 = (e & 1) ? bfhi(pp[0][m][w]) + bfhi(pp[1][m][w]) : bflo(pp[0][m][w]) + bflo(pp[1][m][w]);
            const float y2 = (e & 1) ? bfhi(pp[2][m][w]) + bfhi(pp[3][m][w]) : bflo(pp[2][m][w]) + bflo(pp[3][m][w]);
            r[e] = gt * acc[m][e >> 2][e & 3] + og * (y1 - y2);
        }
        u32x4 v; v[0] = cvtpk(r[0], r[1]); v[1] = cvtpk(r[2], r[3]); v[2] = cvtpk(r[4], r[5]); v[3] = cvtpk(r[6], r[7]);
        *(u32x4*)(out + (size_t)(tb * 64 + wt * 32 + m * 16 + i16) * DM + eb * 64 + we * 32 + g * 8) = v;
    }
    STAMP(2, 1);
}

#define PROF_PASS
extern "C" void kernel_launch(void* const* d_in, const int* in_sizes, int n_in, void* d_out, int out_size, void* d_ws, size_t ws_size,
                              hipStream_t stream) {
    const u16* x = (const u16*)d_in[0]; const u16* wqkv = (const u16*)d_in[1]; const u16* wo = (const u16*)d_in[2];
    const u16* cre = (const u16*)d_in[4]; const u16* cim = (const u16*)d_in[5];
    const u16* hre = (const u16*)d_in[6]; const u16* him = (const u16*)d_in[7]; const u16* gw = (const u16*)d_in[8];
    char* ws = (char*)d_ws + (WS_END ? ((ws_size - (24u << 20)) & ~(size_t)0xfffff) : 0);
    (void)hipFuncSetAttribute((const void*)k1_proj, hipFuncAttributeMaxDynamicSharedMemorySize, K1_LDS);
    (void)hipFuncSetAttribute((const void*)k2_mix, hipFuncAttributeMaxDynamicSharedMemorySize, K2_LDS);
    (void)hipFuncSetAttribute((const void*)k3_out, hipFuncAttributeMaxDynamicSharedMemorySize, K3_LDS);
    k1_proj<<<256, 512, K1_LDS, stream>>>(x, wqkv, wo, hre, him, cre, cim, gw, ws PROF_PASS);
    k2_mix<<<256, 512, K2_LDS, stream>>>(cre, cim, ws PROF_PASS);
    k3_out<<<256, 256, K3_LDS, stream>>>(ws, (u16*)d_out PROF_PASS);
}
```

```cpp
#define PREFETCH_C 0
#define PROBE 0
#include <hip/hip_runtime.h>
#include <hip/hip_bf16.h>

#define ABL 0
typedef unsigned short u16;
using bf16x8 = __attribute__((ext_vector_type(8))) short;
using s16x4  = __attribute__((ext_vector_type(4))) short;
using f32x16 = __attribute__((ext_vector_type(16))) float;
using f32x4  = __attribute__((ext_vector_type(4))) float;
using u32x4  = __attribute__((ext_vector_type(4))) unsigned;
typedef __bf16 bf2_t __attribute__((ext_vector_type(2)));
typedef float f2_t __attribute__((ext_vector_type(2)));
#define LAS __attribute__((address_space(3)))
#define DI __device__ __forceinline__
#define MFMA32(a, b, c) __builtin_amdgcn_mfma_f32_32x32x16_bf16(a, b, c, 0, 0, 0)
#define MFMA16(a, b, c) __builtin_amdgcn_mfma_f32_16x16x32_bf16(a, b, c, 0, 0, 0)
#define SB() __builtin_amdgcn_sched_barrier(0)
#define BAR() asm volatile("s_barrier" ::: "memory")
#define PIN(x) asm volatile("" : "+v"(x))
#define LGKM0() __builtin_amdgcn_s_waitcnt(0xC07F)
#define WAIT_L0() asm volatile("s_waitcnt lgkmcnt(0)" ::: "memory")
template <int N> DI void wait_v() { asm volatile("s_waitcnt vmcnt(%0)" ::"n"(N) : "memory"); }
typedef __amdgpu_buffer_rsrc_t rsrc_t;
DI rsrc_t mk_rsrc(const void* p, unsigned bytes) { return __builtin_amdgcn_make_buffer_rsrc((void*)p, 0, bytes, 0x00020000); }
template <int AUX = 0> DI void glds(rsrc_t r, LAS char* dst, unsigned voff, unsigned soff) {
    __builtin_amdgcn_raw_ptr_buffer_load_lds(r, (LAS void*)dst, 16, voff, soff, 0, AUX);
}
#define AUX_C 0
#define AUX_PF 0
#define AUX_IN 0
#define WS_END 0
DI unsigned cvtpk(float lo, float hi) { f2_t v = {lo, hi}; bf2_t r = __builtin_convertvector(v, bf2_t); return __builtin_bit_cast(unsigned, r); }
DI u32x4 pack8(const f32x4& a, const f32x4& b) { u32x4 v; v[0] = cvtpk(a[0], a[1]); v[1] = cvtpk(a[2], a[3]); v[2] = cvtpk(b[0], b[1]); v[3] = cvtpk(b[2], b[3]); return v; }
DI float bflo(unsigned u) { return __uint_as_float(u << 16); }
DI float bfhi(unsigned u) { return __uint_as_float(u & 0xffff0000u); }
DI float bfround(float f) { return bflo(cvtpk(f, f)); }
DI LAS char* LP(unsigned a) { return (LAS char*)(size_t)a; }
DI unsigned LA(LAS char* p) { return (unsigned)(size_t)p; }
DI bf16x8 ldsr(LAS char* p) { return *(LAS bf16x8*)p; }
DI s16x4 ldtr(LAS char* p) { return __builtin_amdgcn_ds_read_tr16_b64_v4i16((LAS s16x4*)p); }
DI bf16x8 ldtr2(LAS char* p, int off2) { s16x4 a = ldtr(p), b = ldtr(p + off2); return __builtin_shufflevector(a, b, 0, 1, 2, 3, 4, 5, 6, 7); }

#define ST_MODE 0
DI void ST16(void* p, u32x4 v) {
    *(u32x4*)p = v;
}
constexpr int SEQ = 2048, DM = 512;
constexpr size_t WS_Q = 0, WS_K = 2u << 20, WS_V = 4u << 20, WS_AB = 6u << 20, WS_O = 10u << 20, WS_P = 12u << 20, WS_WO = 20u << 20, WS_G = 21u << 20;

#define STAMP(k, i)
#define STAMPW(k, i, t)
#define PROF_ARG
#define PROF_FWD
#define RT(v)
#define CT(v)
#define PUT(k, i, v)
#define PUTC(k, i, v)

#define PB 100
#define PST(i) do { if (blockIdx.x == PB && threadIdx.x == 0) ((volatile unsigned long long*)(ws + (30u << 20)))[i] = __builtin_amdgcn_s_memrealtime(); } while (0)
constexpr int K1_NS = 4, K1_NKT = 8, K1_DUMMY = 147456, K1_LDS = K1_DUMMY + 1024;
#define PF_A -1
#define PF_B 3
template <bool QK>
DI void k1_load(const u16* __restrict__ x, const u16* __restrict__ wqkv, const u16* __restrict__ hmat, const u16* __restrict__ cmat, unsigned coff,
                const u16* __restrict__ gw, const u16* __restrict__ wo, char* __restrict__ ws, LAS char* lds, int rb, int j, int lane, int wid) {
    constexpr int NW = QK ? 4 : 2;
    constexpr int WOFF = 65536, WST = NW * 4096, HOFF = WOFF + 4 * WST, HST = (5 - NW) * 4096;
    const unsigned voffX = (lane >> 3) * 1024 + (((lane & 7) ^ (((lane >> 4) & 1) | (wid << 1))) << 4);
    unsigned voffW;
    if (QK) { const int r = lane >> 4, v = r | (((wid >> 1) & 1) << 2), sw = (v & 1) | ((v >> 1) << 2), c = (lane & 15) ^ sw;
              voffW = r * 3072 + (c >> 3) * 1024 + (c & 7) * 16; }
    else    { const int r = lane >> 3, u = ((r >> 1) & 1) | ((wid & 1) << 1), c = (lane & 7) ^ (u << 1);
              voffW = r * 3072 + c * 16; }
    const unsigned soffX = (unsigned)(rb * 128 + wid * 8) * 1024;
    const unsigned soffW = (unsigned)(wid * (QK ? 4 : 8)) * 3072 + (QK ? 0 : 2048) + j * 128;
    const unsigned soffH = (unsigned)((j & 3) * 128 + (QK ? 0 : 32) + wid * 8) * 1024;
    const rsrc_t rx = mk_rsrc(x, SEQ * DM * 2), rw = mk_rsrc(wqkv, DM * 3 * DM * 2), rh = mk_rsrc(hmat, DM * DM * 2), rc = mk_rsrc(cmat, SEQ * SEQ * 2);
    auto stage = [&](int kt) {
        LAS char* b = lds + wid * 1024; const int sl = kt % K1_NS;
#pragma unroll
        for (int p = 0; p < 9; ++p) {
            if (p < 4)           glds<AUX_IN>(rx, b + sl * 16384 + p * 4096, voffX, soffX + p * 32768 + kt * 128);
            else if (p < 4 + NW) glds<AUX_IN>(rw, b + WOFF + sl * WST + (p - 4) * 4096, voffW, soffW + ((p - 4) * (QK ? 16 : 32) + kt * 64) * 3072);
            else                 glds<AUX_IN>(rh, b + HOFF + sl * HST + (p - 4 - NW) * 4096, voffX, soffH + (p - 4 - NW) * 32768 + kt * 128);
            SB();
        } };
    const unsigned voffC = (lane >> 3) * 4096 + (lane & 7) * 128;
    auto warm = [&](int hh) {
    };
    constexpr int PW = PREFETCH_C ? 1 : 0;
#pragma unroll
    for (int s = 0; s < K1_NS; ++s) stage(s);
    if (PF_A < 0) warm(0);
    if (PF_B < 0) warm(1);
    wait_v<(K1_NS - 1) * 9 + PW * ((PF_A < 0) + (PF_B < 0))>(); BAR();
    u32x4 gx0, gx1, gwv; unsigned wv[8];
#pragma unroll
    for (int kt = 0; kt < K1_NKT - 1; ++kt) {
        constexpr int dummy = 0; (void)dummy;
        const int later = (kt + K1_NS - 1 < K1_NKT ? kt + K1_NS - 1 : K1_NKT - 1) - (kt + 1);
        const int sA = PF_A + K1_NS, sB = PF_B + K1_NS;
        const int wa = (PF_A < kt && sA >= kt + 1) ? PW : 0, wb = (PF_B < kt && sB >= kt + 1) ? PW : 0;
        const int tail = (kt > K1_NKT - K1_NS - 1) ? 11 : 0;
        const int n = later * 9 + wa + wb + tail;
        asm volatile("s_waitcnt vmcnt(%0)" :: "n"(n) : "memory");
        BAR();
        if (kt + K1_NS < K1_NKT && ABL != 3) stage(kt + K1_NS);
        if (kt == K1_NKT - K1_NS - 1) {
            const u16* xr = x + (size_t)(blockIdx.x * 8 + wid * 2) * DM + lane * 8;
            asm volatile("global_load_dwordx4 %0, %1, off" : "=v"(gx0) : "v"(xr));
            asm volatile("global_load_dwordx4 %0, %1, off" : "=v"(gx1) : "v"(xr + DM));
            asm volatile("global_load_dwordx4 %0, %1, off" : "=v"(gwv) : "v"(gw + lane * 8));
            const int g = lane >> 4, i16 = lane & 15, kb = blockIdx.x >> 4, eb = blockIdx.x & 15;
            const u16* wsrc = wo + (size_t)(kb * 32 + (g >> 1) * 16 + (g & 1) * 4) * DM + eb * 32 + 8 * (i16 >> 2) + (i16 & 3) + 4 * (wid & 1);
#pragma unroll
            for (int e = 0; e < 8; ++e) asm volatile("global_load_ushort %0, %1, off" : "=v"(wv[e]) : "v"(wsrc + ((e & 3) + 8 * (e >> 2)) * DM));
        }
        if (kt == PF_A) warm(0);
        if (kt == PF_B) warm(1);
    }
    BAR();
    asm volatile("s_waitcnt vmcnt(%c11)"
                 : "+v"(gx0), "+v"(gx1), "+v"(gwv), "+v"(wv[0]), "+v"(wv[1]), "+v"(wv[2]), "+v"(wv[3]), "+v"(wv[4]), "+v"(wv[5]), "+v"(wv[6]), "+v"(wv[7])
                 : "i"(PW * ((PF_A >= K1_NKT - K1_NS - 1) + (PF_B >= K1_NKT - K1_NS - 1))) : "memory");
    float s0 = 0.f, s1 = 0.f;
#pragma unroll
    for (int e = 0; e < 4; ++e) {
        s0 += bfround(bflo(gx0[e]) * bflo(gwv[e])) + bfround(bfhi(gx0[e]) * bfhi(gwv[e]));
        s1 += bfround(bflo(gx1[e]) * bflo(gwv[e])) + bfround(bfhi(gx1[e]) * bfhi(gwv[e]));
    }
#pragma unroll
    for (int o = 32; o > 0; o >>= 1) { s0 += __shfl_xor(s0, o); s1 += __shfl_xor(s1, o); }
    const float gt = bfround(1.f / (1.f + __expf(-bfround(lane ? s1 : s0))));
    if (lane < 2) ((float*)(ws + WS_G))[blockIdx.x * 8 + wid * 2 + lane] = gt;
    if (wid < 2) {
        u32x4 v;
#pragma unroll
        for (int e = 0; e < 4; ++e) v[e] = wv[2 * e] | (wv[2 * e + 1] << 16);
        *(u32x4*)(ws + WS_WO + (size_t)(blockIdx.x * 2 + wid) * 1024 + lane * 16) = v;
    }
}

template <bool QK>
DI void k1_mma(char* __restrict__ ws, LAS char* lds, int rb, int j, int lane, int wid PROF_ARG) {
    constexpr int NW = QK ? 4 : 2;
    constexpr int NH = QK ? 1 : 3;
    constexpr int WOFF = 65536, WST = NW * 4096, HOFF = WOFF + 4 * WST, HST = NH * 4096;
    constexpr int TOFF = QK ? 1024 : 512, KSOFF = QK ? 8192 : 4096;
    const int wm = wid >> 1, wn = wid & 1, i16 = lane & 15, g = lane >> 4;
    unsigned ax[2], ah[2];
#pragma unroll
    for (int ks = 0; ks < 2; ++ks) {
        const unsigned a = (8 * (i16 >> 2) + (i16 & 3)) * 128 + (((4 * ks + g) ^ (i16 >> 1)) << 4);
        ax[ks] = LA(lds) + a + wm * 8192; ah[ks] = LA(lds) + a + HOFF + wn * 512; }
    unsigned aw[NW];
    { const int q = i16 >> 2, p = lane & 3;
      if (QK) { const int v = q | ((g & 1) << 2), sw = (v & 1) | ((v >> 1) << 2);
#pragma unroll
          for (int f = 0; f < NW; ++f) aw[f] = LA(lds) + WOFF + (8 * g + q) * 256 + (((8 * wn + 4 * (f >> 1) + (f & 1) + 2 * (p >> 1)) ^ sw) << 4) + 8 * (p & 1);
      } else { const int u = (q >> 1) | ((g & 1) << 1);
#pragma unroll
          for (int f = 0; f < NW; ++f) aw[f] = LA(lds) + WOFF + (8 * g + q) * 128 + (((4 * wn + 2 * f + (p >> 1)) ^ (u << 1)) << 4) + 8 * (p & 1);
      } }
    f32x4 accw[4][NW], acch[4][NH];
#pragma unroll
    for (int m = 0; m < 4; ++m) {
#pragma unroll
        for (int f = 0; f < NW; ++f) accw[m][f] = f32x4{0.f, 0.f, 0.f, 0.f};
#pragma unroll
        for (int f = 0; f < NH; ++f) acch[m][f] = f32x4{0.f, 0.f, 0.f, 0.f};
    }
    bf16x8 fx[2][4], fh[2][NH]; s16x4 fw[2][NW][2];
    constexpr int NRD = 4 + 2 * NW + NH;
    auto RD = [&](int n, int slot, int sl, int ks) {
        if (n == 0 || n >= NRD - 3) { const int m = n == 0 ? 0 : n - (NRD - 4); fx[slot][m] = ldsr(LP(ax[ks]) + sl * 16384 + (m >> 1) * 4096 + (m & 1) * 512); }
        else if (n - 1 < 2 * NW) { const int f = (n - 1 < 2 * NW) ? (n - 1) >> 1 : 0, t = (n - 1) & 1; fw[slot][f][t] = ldtr(LP(aw[f]) + sl * WST + ks * KSOFF + t * TOFF); }
        else { const int f = (n - 1 >= 2 * NW) ? n - 1 - 2 * NW : 0; fh[slot][f] = ldsr(LP(ah[ks]) + sl * HST + f * 4096); }
    };
    auto HALF = [&](int slot, bool rd, int nbuf, int nks) {
#pragma unroll
        for (int n = 0; n < 20; ++n) {
            const int m = n / 5, f = n % 5;
            if (f < NW) { const int ff = f < NW ? f : 0;
                const bf16x8 w8 = __builtin_shufflevector(fw[slot][ff][0], fw[slot][ff][1], 0, 1, 2, 3, 4, 5, 6, 7);
                if (QK) accw[m][ff] = MFMA16(w8, fx[slot][m], accw[m][ff]); else accw[m][ff] = MFMA16(fx[slot][m], w8, accw[m][ff]); }
            else { const int ff = f >= NW ? f - NW : 0; acch[m][ff] = MFMA16(fx[slot][m], fh[slot][ff], acch[m][ff]); }
            SB();
            if (rd && n < NRD) { RD(n, slot ^ 1, nbuf, nks); SB(); }
        }
        if (rd) { LGKM0(); SB(); } };
#pragma unroll
    for (int f = 0; f < NW; ++f) PIN(aw[f]);
    PIN(ax[0]); PIN(ax[1]); PIN(ah[0]); PIN(ah[1]);
#pragma unroll
    for (int m = 0; m < 4; ++m) {
#pragma unroll
        for (int f = 0; f < NW; ++f) PIN(accw[m][f]);
#pragma unroll
        for (int f = 0; f < NH; ++f) PIN(acch[m][f]);
    }
    RT(t3);
    BAR(); SB();
    RT(t4); CT(c4); PST(1);
#pragma unroll
    for (int n = 0; n < NRD; ++n) RD(n, 0, 0, 0);
    LGKM0(); SB();
    unsigned cs = 0;
#pragma unroll 1
    for (int it = 0; it < 2; ++it) {
#pragma unroll
        for (int sl = 0; sl < 4; ++sl) {
            HALF(0, true, sl, 1);
            BAR(); SB();
            HALF(1, true, (sl + 1) & 3, 0);
        }
    }
    RT(t5); CT(c5);
    PUT(0, 3, t3); PUT(0, 4, t4); PUT(0, 5, t5); PST(2);
    const int tile0 = rb * 4 + wm * 2;
    if (QK) {
        char* dst = ws + (wn ? WS_K : WS_Q) + (size_t)(4 * j + (g >> 1)) * 65536 + tile0 * 1024 + (g & 1) * 512 + ((i16 & 7) + 16 * (i16 >> 3)) * 16;
#pragma unroll
        for (int pr = 0; pr < 2; ++pr)
#pragma unroll
            for (int m = 0; m < 4; ++m)
                ST16(dst + pr * 131072 + (m >> 1) * 1024 + (m & 1) * 128, pack8(accw[m][(2 * pr) % NW], accw[m][(2 * pr + 1) % NW]));
    } else {
        char* dst = ws + WS_V + (size_t)(4 * j + 2 * wn) * 65536 + tile0 * 1024 + lane * 16;
#pragma unroll
        for (int f = 0; f < NW; ++f)
#pragma unroll
            for (int b = 0; b < 2; ++b) ST16(dst + f * 65536 + b * 1024, pack8(accw[2 * b][f], accw[2 * b + 1][f]));
    }
    {
        char* dst = ws + WS_AB + (size_t)((j >> 2) * 64 + tile0) * 32768 + ((j & 3) * 8 + (QK ? 0 : 2) + wn) * 1024 + lane * 16;
#pragma unroll
        for (int f = 0; f < NH; ++f)
#pragma unroll
            for (int b = 0; b < 2; ++b) ST16(dst + b * 32768 + f * 2048, pack8(acch[2 * b][f], acch[2 * b + 1][f]));
    }
}

__global__ __launch_bounds__(512, 2) void k1_proj(const u16* __restrict__ x, const u16* __restrict__ wqkv, const u16* __restrict__ wo,
                                                  const u16* __restrict__ hre, const u16* __restrict__ him, const u16* __restrict__ cre,
                                                  const u16* __restrict__ cim, const u16* __restrict__ gw, char* __restrict__ ws PROF_ARG) {
    extern __shared__ __attribute__((aligned(16))) char smem[];
    STAMP(0, 0); PST(0);
    const int tid = threadIdx.x, lane = tid & 63, wid = __builtin_amdgcn_readfirstlane(tid >> 6);
    const int xcd = blockIdx.x & 7, idx = blockIdx.x >> 3;
    const int rb = (xcd & 1) * 8 + (idx & 7), q4 = xcd >> 1, c4 = idx >> 3, j = q4 * 2 + (c4 >> 1);
    if (wid >= 4) {
        const u16* cm = (q4 >> 1) ? cim : cre; const u16* hm = j < 4 ? hre : him;
        const unsigned coff = (unsigned)((q4 & 1) * 1024 + idx * 32) * 4096 + (xcd & 1) * 2048;
        if (c4 & 1) k1_load<false>(x, wqkv, hm, cm, coff, gw, wo, ws, (LAS char*)smem, rb, j, lane, wid - 4);
        else        k1_load<true>(x, wqkv, hm, cm, coff, gw, wo, ws, (LAS char*)smem, rb, j, lane, wid - 4);
        STAMPW(0, 2, 256);
    } else {
        if (c4 & 1) k1_mma<false>(ws, (LAS char*)smem, rb, j, lane, wid PROF_FWD);
        else        k1_mma<true>(ws, (LAS char*)smem, rb, j, lane, wid PROF_FWD);
        STAMP(0, 1);
        wait_v<0>(); PST(3);
    }
}

#define AT_PRIO 0
#define K2_PRIO 1
constexpr int K2_NS = 3, K2_BOFF = K2_NS * 16384, K2_NKT = 16;
constexpr int K2_LK = 2 * K2_BOFF, K2_LV = K2_LK + 16384, K2_L1 = K2_LV + 16384, K2_LDS = K2_L1 + 16384;

DI void k2_gemm(const u16* __restrict__ cmat, char* __restrict__ ws, LAS char* lds, int ksl, int tp, int nt, int lane, int wid PROF_ARG) {
    const int wm = wid >> 1, wn = wid & 1, i32 = lane & 31, h = lane >> 5;
    const unsigned voffA = (lane >> 3) * 4096 + (((lane & 7) ^ (4 * (wid & 1) + (lane >> 4))) << 4);
    const unsigned voffB = lane * 16;
    const unsigned soffA = (unsigned)(tp * 128 + wid * 8) * 4096 + (ksl & 1) * 2048;
    const unsigned soffB = (unsigned)(ksl * 32 + (wid >> 1)) * 32768 + (nt * 8 + (wid & 1) * 4) * 1024;
    const char* abp = ws + WS_AB;
    auto piece = [&](int p, int kt, int sl) {
        const bool ok = kt < K2_NKT;
        if ((p & 1) == 0) glds<AUX_C>(mk_rsrc(cmat, ok ? SEQ * SEQ * 2 : 0), lds + sl * 16384 + wid * 1024 + (p >> 1) * 4096, voffA, soffA + (p >> 1) * 131072 + kt * 128);
        else              glds(mk_rsrc(abp, ok ? 4u << 20 : 0), lds + K2_BOFF + sl * 16384 + wid * 4096 + (p >> 1) * 1024, voffB, soffB + kt * 65536 + (p >> 1) * 1024);
    };
    unsigned ac[4];
#pragma unroll
    for (int ks = 0; ks < 4; ++ks) ac[ks] = LA(lds) + (wm * 64 + i32) * 128 + (((2 * ks + h) ^ ((i32 >> 1) & 7)) << 4);
    unsigned ab = LA(lds) + K2_BOFF + wn * 4096 + (i32 >> 4) * 1024 + h * 256 + (i32 & 15) * 16;
    f32x16 acc[2][2];
#pragma unroll
    for (int m = 0; m < 2; ++m)
#pragma unroll
        for (int n = 0; n < 2; ++n)
#pragma unroll
            for (int r = 0; r < 16; ++r) acc[m][n][r] = 0.f;
    bf16x8 fc[2][2], fb[2][2];
    auto RDC = [&](int slot, int m, int sl, int ks) { fc[slot][m] = ldsr(LP(ac[ks]) + sl * 16384 + m * 4096); };
    auto RDB = [&](int slot, int n, int sl, int ks) { fb[slot][n] = ldsr(LP(ab) + sl * 16384 + (ks >> 1) * 8192 + (ks & 1) * 512 + n * 2048); };
    auto QUART = [&](int slot, int nbuf, int nks, int p0, int kt2, int buf2) {
        acc[0][0] = MFMA32(fb[slot][0], fc[slot][0], acc[0][0]); SB();
        RDB(slot ^ 1, 0, nbuf, nks); RDC(slot ^ 1, 0, nbuf, nks); SB();
        acc[0][1] = MFMA32(fb[slot][1], fc[slot][0], acc[0][1]); SB();
        RDB(slot ^ 1, 1, nbuf, nks); RDC(slot ^ 1, 1, nbuf, nks); SB();
        acc[1][1] = MFMA32(fb[slot][1], fc[slot][1], acc[1][1]); SB();
        if (p0 >= 0) { piece(p0, kt2, buf2); SB(); }
        acc[1][0] = MFMA32(fb[slot][0], fc[slot][1], acc[1][0]); SB();
        if (p0 >= 0) { piece(p0 + 1, kt2, buf2); SB(); }
    };
#pragma unroll
    for (int s = 0; s < K2_NS - 1; ++s)
#pragma unroll
        for (int p = 0; p < 8; ++p) piece(p, s, s);
    piece(0, K2_NS - 1, K2_NS - 1); piece(1, K2_NS - 1, K2_NS - 1);
    __builtin_amdgcn_s_setprio(K2_PRIO);
    PIN(ac[0]); PIN(ac[1]); PIN(ac[2]); PIN(ac[3]); PIN(ab);
    PIN(acc[0][0]); PIN(acc[0][1]); PIN(acc[1][0]); PIN(acc[1][1]);
    RT(t3);
    wait_v<(K2_NS - 2) * 8 + 2>(); BAR(); SB();
    RT(t4); CT(c4);
    unsigned long long pt5 = __builtin_amdgcn_s_memrealtime(); LGKM0();
    RDB(0, 0, 0, 0); RDC(0, 0, 0, 0); RDB(0, 1, 0, 0); RDC(0, 1, 0, 0); LGKM0(); SB();
    unsigned cs = 0;
#pragma unroll 1
    for (int k0 = 0; k0 < K2_NKT - 1; k0 += 3) {
#pragma unroll
        for (int sl = 0; sl < 3; ++sl) {
            const int kt = k0 + sl;
            QUART(0, sl, 1, 2, kt + 2, (sl + 2) % 3);
            QUART(1, sl, 2, 4, kt + 2, (sl + 2) % 3);
            QUART(0, sl, 3, 6, kt + 2, (sl + 2) % 3);
            wait_v<8>(); LGKM0(); BAR(); SB();
            QUART(1, (sl + 1) % 3, 0, 0, kt + 3, sl);
        }
    }
    QUART(0, 0, 1, -1, 0, 0); QUART(1, 0, 2, -1, 0, 0); QUART(0, 0, 3, -1, 0, 0); QUART(1, 0, 0, -1, 0, 0);
    RT(t5); CT(c5);
    PUT(1, 3, t3); PUT(1, 4, t4); PUT(1, 5, t5); PST(6);
    if (blockIdx.x == PB && threadIdx.x == 0) ((volatile unsigned long long*)(ws + (30u << 20)))[5] = pt5;
    char* dst = ws + WS_P + (size_t)ksl * (2u << 20) + (size_t)(tp * 8 + wm * 4 + (i32 >> 4)) * 16384 + (nt * 4 + wn * 2) * 1024 + h * 256 + (i32 & 15) * 16;
#pragma unroll
    for (int m = 0; m < 2; ++m)
#pragma unroll
        for (int n = 0; n < 2; ++n)
#pragma unroll
            for (int b = 0; b < 2; ++b) {
                const f32x16& a = acc[m][n];
                u32x4 v; v[0] = cvtpk(a[4 * b], a[4 * b + 1]); v[1] = cvtpk(a[4 * b + 2], a[4 * b + 3]);
                v[2] = cvtpk(a[8 + 4 * b], a[9 + 4 * b]); v[3] = cvtpk(a[10 + 4 * b], a[11 + 4 * b]);
                ST16(dst + m * 32768 + n * 1024 + b * 512, v);
            }
}

#define MX3(a, b, c) __builtin_fmaxf(__builtin_fmaxf((a), (b)), (c))
#define PACE() do { SB(); BAR(); SB(); } while (0)
DI void k2_attn(char* __restrict__ ws, LAS char* lds, int head, int qb, int lane, int aw) {
    const int i32 = lane & 31, h = lane >> 5;
    {
        const unsigned vo = lane * 16;
#pragma unroll
        for (int i = 0; i < 4; ++i) {
            const int t = aw + 4 * i; const int gt = qb * 8 - 8 + t; const bool ok = gt >= 0;
            const unsigned so = (unsigned)head * 65536 + (ok ? gt : 0) * 1024;
            glds(mk_rsrc(ws + WS_K, ok ? 2u << 20 : 0), lds + K2_LK + t * 1024, vo, so);
            glds(mk_rsrc(ws + WS_V, ok ? 2u << 20 : 0), lds + K2_LV + t * 1024, vo, so);
        }
    }
    const int qt0 = qb * 8 + aw * 2;
    bf16x8 q0 = *(const bf16x8*)(ws + WS_Q + (size_t)head * 65536 + qt0 * 1024 + lane * 16);
    bf16x8 q1 = *(const bf16x8*)(ws + WS_Q + (size_t)head * 65536 + qt0 * 1024 + 1024 + lane * 16);
    if (aw == 0) {
        const u32x4 ones = {0x3f803f80u, 0x3f803f80u, 0x3f803f80u, 0x3f803f80u};
        *(LAS u32x4*)(lds + K2_L1 + i32 * 512 + h * 64) = ones;
    }
    __builtin_amdgcn_s_setprio(AT_PRIO);
    BAR();
    wait_v<0>(); WAIT_L0(); BAR();
    const int ql = (i32 & 19) | ((i32 & 4) << 1) | ((i32 & 8) >> 1);
    const int thr = ql - 8 * h;
    const unsigned kaddr = K2_LK + aw * 2048 + lane * 16;
    const unsigned vaddr = aw * 2048 + (i32 < 16 ? K2_LV + h * 256 + i32 * 16 : K2_L1 + ((i32 >= 20 && i32 < 28) ? 64 : 0));
    constexpr float C2 = 0.25f * 1.4426950408889634f;
    const float NINF = -__builtin_inff();
    f32x16 zero16;
#pragma unroll
    for (int r = 0; r < 16; ++r) zero16[r] = 0.f;
#pragma unroll 1
    for (int it = 0; it < 2; ++it) {
        const bf16x8 qf = it ? q1 : q0;
        const int qt = qt0 + it;
        LAS char* kp = lds + kaddr + it * 1024;
        LAS char* vp = lds + vaddr + it * 1024;
        f32x16 s[8];
        {
            bf16x8 kf[9];
#pragma unroll
            for (int t = 0; t < 9; ++t) kf[t] = ldsr(kp + (t == 0 ? 8 : t - 1) * 1024);
            SB();
            f32x16 s8 = MFMA32(kf[0], qf, zero16);
#pragma unroll
            for (int t = 0; t < 8; ++t) s[t] = MFMA32(kf[t + 1], qf, zero16);
            if (qt < 8) {
#pragma unroll
                for (int t = 0; t < 8; ++t) if (qt + t < 8) {
#pragma unroll
                    for (int r = 0; r < 16; ++r) s[t][r] = NINF; }
            }
#pragma unroll
            for (int r = 0; r < 16; ++r) s[0][r] = (16 * (r >> 3) + (r & 7) > thr) ? s[0][r] : s8[r];
            PIN(s[0]);
        }
        PACE();
#pragma unroll
        for (int t = 0; t < 8; ++t) PIN(s[t]);
        float m;
        {
            float ma = MX3(s[0][0], s[0][1], s[0][2]), mb = s[0][3];
#pragma unroll
            for (int e = 4; e < 128; e += 4) {
                ma = MX3(ma, s[e >> 4][e & 15], s[e >> 4][(e & 15) + 1]); mb = MX3(mb, s[e >> 4][(e & 15) + 2], s[e >> 4][(e & 15) + 3]); }
            m = __builtin_fmaxf(ma, mb);
            auto rr = __builtin_amdgcn_permlane32_swap(__float_as_uint(m), __float_as_uint(m), false, false);
            m = __builtin_fmaxf(__uint_as_float(rr[0]), __uint_as_float(rr[1]));
        }
        float nm = -m * C2;
        f32x16 o = zero16;
#pragma unroll
        for (int u = 0; u < 16; ++u) {
            const int t = u >> 1, r0 = (u & 1) * 8;
            const bf16x8 vf = ldsr(vp + t * 1024 + (u & 1) * 512);
            u32x4 p;
#pragma unroll
            for (int r = 0; r < 8; r += 2) {
                const float e0 = __builtin_amdgcn_exp2f(__builtin_fmaf(s[t][r0 + r], C2, nm)), e1 = __builtin_amdgcn_exp2f(__builtin_fmaf(s[t][r0 + r + 1], C2, nm));
                p[r >> 1] = cvtpk(e0, e1);
            }
            if (t == 0) {
                u32x4 p8;
#pragma unroll
                for (int r = 0; r < 8; r += 2) {
                    const int k0 = 2 * r0 + r;
                    const unsigned mk = (k0 > thr ? 0xffffu : 0u) | (k0 + 1 > thr ? 0xffff0000u : 0u);
                    p8[r >> 1] = p[r >> 1] & ~mk; p[r >> 1] &= mk;
                }
                o = MFMA32(ldsr(vp + 8 * 1024 + (u & 1) * 512), __builtin_bit_cast(bf16x8, p8), o);
            }
            o = MFMA32(vf, __builtin_bit_cast(bf16x8, p), o);
            if (u % 3 == 0 && u < 15) { PACE(); PIN(nm); }
        }
        PACE();
        const float rl = __builtin_amdgcn_rcpf(o[8]);
        u32x4 ov; ov[0] = cvtpk(o[0] * rl, o[1] * rl); ov[1] = cvtpk(o[2] * rl, o[3] * rl); ov[2] = cvtpk(o[4] * rl, o[5] * rl); ov[3] = cvtpk(o[6] * rl, o[7] * rl);
        ST16(ws + WS_O + (size_t)(qt * 2 + (ql >> 4)) * 16384 + (head >> 1) * 1024 + ((head & 1) * 2 + h) * 256 + (ql & 15) * 16, ov);
    }
}

__global__ __launch_bounds__(512, 2) void k2_mix(const u16* __restrict__ cre, const u16* __restrict__ cim, char* __restrict__ ws PROF_ARG) {
    extern __shared__ __attribute__((aligned(16))) char smem[];
    STAMP(1, 0); PST(4);
    const int tid = threadIdx.x, lane = tid & 63, wid = __builtin_amdgcn_readfirstlane(tid >> 6);
    const int xcd = blockIdx.x & 7, idx = blockIdx.x >> 3, q4 = xcd >> 1, rh = xcd & 1;
    if (wid < 4) {
        k2_gemm((q4 >> 1) ? cim : cre, ws, (LAS char*)smem, (q4 >> 1) * 2 + rh, (q4 & 1) * 8 + (idx >> 2), idx & 3, lane, wid PROF_FWD);
        STAMP(1, 1);
        wait_v<0>(); PST(7);
    } else {
        if (ABL != 5) k2_attn(ws, (LAS char*)smem, q4 * 8 + (idx >> 2), rh * 4 + (idx & 3), lane, wid - 4);
        STAMPW(1, 2, 256);
    }
}

constexpr int K3_LDS = 131072;
__global__ __launch_bounds__(256, 2) void k3_out(const char* __restrict__ ws, u16* __restrict__ out PROF_ARG) {
    extern __shared__ __attribute__((aligned(16))) char smem[];
    STAMP(2, 0);
    const unsigned long long p8 = __builtin_amdgcn_s_memrealtime();
    LAS char* lds = (LAS char*)smem;
    const int tid = threadIdx.x, lane = tid & 63, wid = __builtin_amdgcn_readfirstlane(tid >> 6);
    const int xcd = blockIdx.x & 7, idx = blockIdx.x >> 3;
    const int tb = xcd * 4 + (idx >> 3), eb = idx & 7;
    const int wt = wid >> 1, we = wid & 1, i16 = lane & 15, g = lane >> 4;
    const rsrc_t ro = mk_rsrc(ws + WS_O, 2u << 20), rw = mk_rsrc(ws + WS_WO, 512u << 10);
    const unsigned vo = lane * 16, so = (unsigned)(tb * 4 + wid) * 16384, sw = (unsigned)(eb * 4 + wid) * 1024;
#pragma unroll
    for (int kb = 0; kb < 16; ++kb) {
        glds(ro, lds + kb * 8192 + wid * 1024, vo, so + kb * 1024);
        glds(rw, lds + kb * 8192 + 4096 + wid * 1024, vo, sw + kb * 32768);
    }
    u32x4 pp[4][2]; float gg[2];
#pragma unroll
    for (int m = 0; m < 2; ++m) {
        const char* src = ws + WS_P + (size_t)(tb * 4 + wt * 2 + m) * 16384 + (eb * 2 + we) * 1024 + lane * 16;
#pragma unroll
        for (int k = 0; k < 4; ++k) asm volatile("global_load_dwordx4 %0, %1, off" : "=v"(pp[k][m]) : "v"(src + (size_t)k * (2u << 20)));
        asm volatile("global_load_dword %0, %1, off" : "=v"(gg[m]) : "v"((const float*)(ws + WS_G) + tb * 64 + wt * 32 + m * 16 + i16));
    }
    f32x4 acc[2][2];
#pragma unroll
    for (int m = 0; m < 2; ++m) { acc[m][0] = f32x4{0.f, 0.f, 0.f, 0.f}; acc[m][1] = f32x4{0.f, 0.f, 0.f, 0.f}; }
    LAS char* pa = lds + wt * 2048 + lane * 16;
    LAS char* pb = lds + 4096 + we * 2048 + lane * 16;
#pragma unroll
    for (int ph = 0; ph < 4; ++ph) {
        if (ph == 0) wait_v<34>(); else if (ph == 1) wait_v<26>(); else if (ph == 2) wait_v<18>(); else wait_v<10>();
        BAR(); SB();
#pragma unroll
        for (int kk = 0; kk < 4; ++kk) {
            const int kb = ph * 4 + kk;
            const bf16x8 o0 = ldsr(pa + kb * 8192), o1 = ldsr(pa + kb * 8192 + 1024), w0 = ldsr(pb + kb * 8192), w1 = ldsr(pb + kb * 8192 + 1024);
            acc[0][0] = MFMA16(w0, o0, acc[0][0]); acc[0][1] = MFMA16(w1, o0, acc[0][1]);
            acc[1][0] = MFMA16(w0, o1, acc[1][0]); acc[1][1] = MFMA16(w1, o1, acc[1][1]);
        }
        SB();
    }
    asm volatile("s_waitcnt vmcnt(0)" : "+v"(pp[0][0]), "+v"(pp[1][0]), "+v"(pp[2][0]), "+v"(pp[3][0]), "+v"(pp[0][1]), "+v"(pp[1][1]), "+v"(pp[2][1]), "+v"(pp[3][1]),
                 "+v"(gg[0]), "+v"(gg[1]) :: "memory");
#pragma unroll
    for (int m = 0; m < 2; ++m) {
        const float gt = gg[m], og = bfround(1.f - gt);
        float r[8];
#pragma unroll
        for (int e = 0; e < 8; ++e) {
            const int w = e >> 1;
            const float y1 = (e & 1) ? bfhi(pp[0][m][w]) + bfhi(pp[1][m][w]) : bflo(pp[0][m][w]) + bflo(pp[1][m][w]);
            const float y2 = (e & 1) ? bfhi(pp[2][m][w]) + bfhi(pp[3][m][w]) : bflo(pp[2][m][w]) + bflo(pp[3][m][w]);
            r[e] = gt * acc[m][e >> 2][e & 3] + og * (y1 - y2);
        }
        u32x4 v; v[0] = cvtpk(r[0], r[1]); v[1] = cvtpk(r[2], r[3]); v[2] = cvtpk(r[4], r[5]); v[3] = cvtpk(r[6], r[7]);
        *(u32x4*)(out + (size_t)(tb * 64 + wt * 32 + m * 16 + i16) * DM + eb * 64 + we * 32 + g * 8) = v;
    }
    STAMP(2, 1);
    if (blockIdx.x == PB && threadIdx.x == 0) {
        wait_v<0>();
        const unsigned long long p9 = __builtin_amdgcn_s_memrealtime();
        const volatile unsigned long long* st = (const volatile unsigned long long*)(ws + (30u << 20));
        const unsigned long long a = st[0], b = PROBE == 8 ? p8 : PROBE == 9 ? p9 : st[PROBE];
        unsigned long long d = (b - a) * 4; if (d > 40000) d = 40000;
        while (__builtin_amdgcn_s_memrealtime() - p9 < d) __builtin_amdgcn_s_sleep(1);
    }
}

#define PROF_PASS
extern "C" void kernel_launch(void* const* d_in, const int* in_sizes, int n_in, void* d_out, int out_size, void* d_ws, size_t ws_size,
                              hipStream_t stream) {
    const u16* x = (const u16*)d_in[0]; const u16* wqkv = (const u16*)d_in[1]; const u16* wo = (const u16*)d_in[2];
    const u16* cre = (const u16*)d_in[4]; const u16* cim = (const u16*)d_in[5];
    const u16* hre = (const u16*)d_in[6]; const u16* him = (const u16*)d_in[7]; const u16* gw = (const u16*)d_in[8];
    char* ws = (char*)d_ws + (WS_END ? ((ws_size - (24u << 20)) & ~(size_t)0xfffff) : 0);
    (void)hipFuncSetAttribute((const void*)k1_proj, hipFuncAttributeMaxDynamicSharedMemorySize, K1_LDS);
    (void)hipFuncSetAttribute((const void*)k2_mix, hipFuncAttributeMaxDynamicSharedMemorySize, K2_LDS);
    (void)hipFuncSetAttribute((const void*)k3_out, hipFuncAttributeMaxDynamicSharedMemorySize, K3_LDS);
    k1_proj<<<256, 512, K1_LDS, stream>>>(x, wqkv, wo, hre, him, cre, cim, gw, ws PROF_PASS);
    k2_mix<<<256, 512, K2_LDS, stream>>>(cre, cim, ws PROF_PASS);
    k3_out<<<256, 256, K3_LDS, stream>>>(ws, (u16*)d_out PROF_PASS);
}
```

```cpp
#define PREFETCH_C 0
#define PROBE 0
#include <hip/hip_runtime.h>
#include <hip/hip_bf16.h>

#define ABL 0
typedef unsigned short u16;
using bf16x8 = __attribute__((ext_vector_type(8))) short;
using s16x4  = __attribute__((ext_vector_type(4))) short;
using f32x16 = __attribute__((ext_vector_type(16))) float;
using f32x4  = __attribute__((ext_vector_type(4))) float;
using u32x4  = __attribute__((ext_vector_type(4))) unsigned;
typedef __bf16 bf2_t __attribute__((ext_vector_type(2)));
typedef float f2_t __attribute__((ext_vector_type(2)));
#define LAS __attribute__((address_space(3)))
#define DI __device__ __forceinline__
#define MFMA32(a, b, c) __builtin_amdgcn_mfma_f32_32x32x16_bf16(a, b, c, 0, 0, 0)
#define MFMA16(a, b, c) __builtin_amdgcn_mfma_f32_16x16x32_bf16(a, b, c, 0, 0, 0)
#define SB() __builtin_amdgcn_sched_barrier(0)
#define BAR() asm volatile("s_barrier" ::: "memory")
#define PIN(x) asm volatile("" : "+v"(x))
#define LGKM0() __builtin_amdgcn_s_waitcnt(0xC07F)
#define WAIT_L0() asm volatile("s_waitcnt lgkmcnt(0)" ::: "memory")
template <int N> DI void wait_v() { asm volatile("s_waitcnt vmcnt(%0)" ::"n"(N) : "memory"); }
typedef __amdgpu_buffer_rsrc_t rsrc_t;
DI rsrc_t mk_rsrc(const void* p, unsigned bytes) { return __builtin_amdgcn_make_buffer_rsrc((void*)p, 0, bytes, 0x00020000); }
template <int AUX = 0> DI void glds(rsrc_t r, LAS char* dst, unsigned voff, unsigned soff) {
    __builtin_amdgcn_raw_ptr_buffer_load_lds(r, (LAS void*)dst, 16, voff, soff, 0, AUX);
}
#define AUX_C 0
#define AUX_PF 0
#define AUX_IN 0
#define WS_END 0
DI unsigned cvtpk(float lo, float hi) { f2_t v = {lo, hi}; bf2_t r = __builtin_convertvector(v, bf2_t); return __builtin_bit_cast(unsigned, r); }
DI u32x4 pack8(const f32x4& a, const f32x4& b) { u32x4 v; v[0] = cvtpk(a[0], a[1]); v[1] = cvtpk(a[2], a[3]); v[2] = cvtpk(b[0], b[1]); v[3] = cvtpk(b[2], b[3]); return v; }
DI float bflo(unsigned u) { return __uint_as_float(u << 16); }
DI float bfhi(unsigned u) { return __uint_as_float(u & 0xffff0000u); }
DI float bfround(float f) { return bflo(cvtpk(f, f)); }
DI LAS char* LP(unsigned a) { return (LAS char*)(size_t)a; }
DI unsigned LA(LAS char* p) { return (unsigned)(size_t)p; }
DI bf16x8 ldsr(LAS char* p) { return *(LAS bf16x8*)p; }
DI s16x4 ldtr(LAS char* p) { return __builtin_amdgcn_ds_read_tr16_b64_v4i16((LAS s16x4*)p); }
DI bf16x8 ldtr2(LAS char* p, int off2) { s16x4 a = ldtr(p), b = ldtr(p + off2); return __builtin_shufflevector(a, b, 0, 1, 2, 3, 4, 5, 6, 7); }

#define ST_MODE 0
DI void ST16(void* p, u32x4 v) {
    *(u32x4*)p = v;
}
constexpr int SEQ = 2048, DM = 512;
constexpr size_t WS_Q = 0, WS_K = 2u << 20, WS_V = 4u << 20, WS_AB = 6u << 20, WS_O = 10u << 20, WS_P = 12u << 20, WS_WO = 20u << 20, WS_G = 21u << 20;

#define STAMP(k, i)
#define STAMPW(k, i, t)
#define PROF_ARG
#define PROF_FWD
#define RT(v)
#define CT(v)
#define PUT(k, i, v)
#define PUTC(k, i, v)

#define PB 100
#define PST(i) do { if (blockIdx.x == PB && threadIdx.x == 0) ((volatile unsigned long long*)(ws + (30u << 20)))[i] = __builtin_amdgcn_s_memrealtime(); } while (0)
constexpr int K1_NS = 4, K1_NKT = 8, K1_DUMMY = 147456, K1_LDS = K1_DUMMY + 1024;
#define PF_A -1
#define PF_B 3
template <bool QK>
DI void k1_load(const u16* __restrict__ x, const u16* __restrict__ wqkv, const u16* __restrict__ hmat, const u16* __restrict__ cmat, unsigned coff,
                const u16* __restrict__ gw, const u16* __restrict__ wo, char* __restrict__ ws, LAS char* lds, int rb, int j, int lane, int wid) {
    constexpr int NW = QK ? 4 : 2;
    constexpr int WOFF = 65536, WST = NW * 4096, HOFF = WOFF + 4 * WST, HST = (5 - NW) * 4096;
    const unsigned voffX = (lane >> 3) * 1024 + (((lane & 7) ^ (((lane >> 4) & 1) | (wid << 1))) << 4);
    unsigned voffW;
    if (QK) { const int r = lane >> 4, v = r | (((wid >> 1) & 1) << 2), sw = (v & 1) | ((v >> 1) << 2), c = (lane & 15) ^ sw;
              voffW = r * 3072 + (c >> 3) * 1024 + (c & 7) * 16; }
    else    { const int r = lane >> 3, u = ((r >> 1) & 1) | ((wid & 1) << 1), c = (lane & 7) ^ (u << 1);
              voffW = r * 3072 + c * 16; }
    const unsigned soffX = (unsigned)(rb * 128 + wid * 8) * 1024;
    const unsigned soffW = (unsigned)(wid * (QK ? 4 : 8)) * 3072 + (QK ? 0 : 2048) + j * 128;
    const unsigned soffH = (unsigned)((j & 3) * 128 + (QK ? 0 : 32) + wid * 8) * 1024;
    const rsrc_t rx = mk_rsrc(x, SEQ * DM * 2), rw = mk_rsrc(wqkv, DM * 3 * DM * 2), rh = mk_rsrc(hmat, DM * DM * 2), rc = mk_rsrc(cmat, SEQ * SEQ * 2);
    auto stage = [&](int kt) {
        LAS char* b = lds + wid * 1024; const int sl = kt % K1_NS;
#pragma unroll
        for (int p = 0; p < 9; ++p) {
            if (p < 4)           glds<AUX_IN>(rx, b + sl * 16384 + p * 4096, voffX, soffX + p * 32768 + kt * 128);
            else if (p < 4 + NW) glds<AUX_IN>(rw, b + WOFF + sl * WST + (p - 4) * 4096, voffW, soffW + ((p - 4) * (QK ? 16 : 32) + kt * 64) * 3072);
            else                 glds<AUX_IN>(rh, b + HOFF + sl * HST + (p - 4 - NW) * 4096, voffX, soffH + (p - 4 - NW) * 32768 + kt * 128);
            SB();
        } };
    const unsigned voffC = (lane >> 3) * 4096 + (lane & 7) * 128;
    auto warm = [&](int hh) {
    };
    constexpr int PW = PREFETCH_C ? 1 : 0;
#pragma unroll
    for (int s = 0; s < K1_NS; ++s) stage(s);
    if (PF_A < 0) warm(0);
    if (PF_B < 0) warm(1);
    wait_v<(K1_NS - 1) * 9 + PW * ((PF_A < 0) + (PF_B < 0))>(); BAR();
    u32x4 gx0, gx1, gwv; unsigned wv[8];
#pragma unroll
    for (int kt = 0; kt < K1_NKT - 1; ++kt) {
        constexpr int dummy = 0; (void)dummy;
        const int later = (kt + K1_NS - 1 < K1_NKT ? kt + K1_NS - 1 : K1_NKT - 1) - (kt + 1);
        const int sA = PF_A + K1_NS, sB = PF_B + K1_NS;
        const int wa = (PF_A < kt && sA >= kt + 1) ? PW : 0, wb = (PF_B < kt && sB >= kt + 1) ? PW : 0;
        const int tail = (kt > K1_NKT - K1_NS - 1) ? 11 : 0;
        const int n = later * 9 + wa + wb + tail;
        asm volatile("s_waitcnt vmcnt(%0)" :: "n"(n) : "memory");
        BAR();
        if (kt + K1_NS < K1_NKT && ABL != 3) stage(kt + K1_NS);
        if (kt == K1_NKT - K1_NS - 1) {
            const u16* xr = x + (size_t)(blockIdx.x * 8 + wid * 2) * DM + lane * 8;
            asm volatile("global_load_dwordx4 %0, %1, off" : "=v"(gx0) : "v"(xr));
            asm volatile("global_load_dwordx4 %0, %1, off" : "=v"(gx1) : "v"(xr + DM));
            asm volatile("global_load_dwordx4 %0, %1, off" : "=v"(gwv) : "v"(gw + lane * 8));
            const int g = lane >> 4, i16 = lane & 15, kb = blockIdx.x >> 4, eb = blockIdx.x & 15;
            const u16* wsrc = wo + (size_t)(kb * 32 + (g >> 1) * 16 + (g & 1) * 4) * DM + eb * 32 + 8 * (i16 >> 2) + (i16 & 3) + 4 * (wid & 1);
#pragma unroll
            for (int e = 0; e < 8; ++e) asm volatile("global_load_ushort %0, %1, off" : "=v"(wv[e]) : "v"(wsrc + ((e & 3) + 8 * (e >> 2)) * DM));
        }
        if (kt == PF_A) warm(0);
        if (kt == PF_B) warm(1);
    }
    BAR();
    asm volatile("s_waitcnt vmcnt(%c11)"
                 : "+v"(gx0), "+v"(gx1), "+v"(gwv), "+v"(wv[0]), "+v"(wv[1]), "+v"(wv[2]), "+v"(wv[3]), "+v"(wv[4]), "+v"(wv[5]), "+v"(wv[6]), "+v"(wv[7])
                 : "i"(PW * ((PF_A >= K1_NKT - K1_NS - 1) + (PF_B >= K1_NKT - K1_NS - 1))) : "memory");
    float s0 = 0.f, s1 = 0.f;
#pragma unroll
    for (int e = 0; e < 4; ++e) {
        s0 += bfround(bflo(gx0[e]) * bflo(gwv[e])) + bfround(bfhi(gx0[e]) * bfhi(gwv[e]));
        s1 += bfround(bflo(gx1[e]) * bflo(gwv[e])) + bfround(bfhi(gx1[e]) * bfhi(gwv[e]));
    }
#pragma unroll
    for (int o = 32; o > 0; o >>= 1) { s0 += __shfl_xor(s0, o); s1 += __shfl_xor(s1, o); }
    const float gt = bfround(1.f / (1.f + __expf(-bfround(lane ? s1 : s0))));
    if (lane < 2) ((float*)(ws + WS_G))[blockIdx.x * 8 + wid * 2 + lane] = gt;
    if (wid < 2) {
        u32x4 v;
#pragma unroll
        for (int e = 0; e < 4; ++e) v[e] = wv[2 * e] | (wv[2 * e + 1] << 16);
        *(u32x4*)(ws + WS_WO + (size_t)(blockIdx.x * 2 + wid) * 1024 + lane * 16) = v;
    }
}

template <bool QK>
DI void k1_mma(char* __restrict__ ws, LAS char* lds, int rb, int j, int lane, int wid PROF_ARG) {
    constexpr int NW = QK ? 4 : 2;
    constexpr int NH = QK ? 1 : 3;
    constexpr int WOFF = 65536, WST = NW * 4096, HOFF = WOFF + 4 * WST, HST = NH * 4096;
    constexpr int TOFF = QK ? 1024 : 512, KSOFF = QK ? 8192 : 4096;
    const int wm = wid >> 1, wn = wid & 1, i16 = lane & 15, g = lane >> 4;
    unsigned ax[2], ah[2];
#pragma unroll
    for (int ks = 0; ks < 2; ++ks) {
        const unsigned a = (8 * (i16 >> 2) + (i16 & 3)) * 128 + (((4 * ks + g) ^ (i16 >> 1)) << 4);
        ax[ks] = LA(lds) + a + wm * 8192; ah[ks] = LA(lds) + a + HOFF + wn * 512; }
    unsigned aw[NW];
    { const int q = i16 >> 2, p = lane & 3;
      if (QK) { const int v = q | ((g & 1) << 2), sw = (v & 1) | ((v >> 1) << 2);
#pragma unroll
          for (int f = 0; f < NW; ++f) aw[f] = LA(lds) + WOFF + (8 * g + q) * 256 + (((8 * wn + 4 * (f >> 1) + (f & 1) + 2 * (p >> 1)) ^ sw) << 4) + 8 * (p & 1);
      } else { const int u = (q >> 1) | ((g & 1) << 1);
#pragma unroll
          for (int f = 0; f < NW; ++f) aw[f] = LA(lds) + WOFF + (8 * g + q) * 128 + (((4 * wn + 2 * f + (p >> 1)) ^ (u << 1)) << 4) + 8 * (p & 1);
      } }
    f32x4 accw[4][NW], acch[4][NH];
#pragma unroll
    for (int m = 0; m < 4; ++m) {
#pragma unroll
        for (int f = 0; f < NW; ++f) accw[m][f] = f32x4{0.f, 0.f, 0.f, 0.f};
#pragma unroll
        for (int f = 0; f < NH; ++f) acch[m][f] = f32x4{0.f, 0.f, 0.f, 0.f};
    }
    bf16x8 fx[2][4], fh[2][NH]; s16x4 fw[2][NW][2];
    constexpr int NRD = 4 + 2 * NW + NH;
    auto RD = [&](int n, int slot, int sl, int ks) {
        if (n == 0 || n >= NRD - 3) { const int m = n == 0 ? 0 : n - (NRD - 4); fx[slot][m] = ldsr(LP(ax[ks]) + sl * 16384 + (m >> 1) * 4096 + (m & 1) * 512); }
        else if (n - 1 < 2 * NW) { const int f = (n - 1 < 2 * NW) ? (n - 1) >> 1 : 0, t = (n - 1) & 1; fw[slot][f][t] = ldtr(LP(aw[f]) + sl * WST + ks * KSOFF + t * TOFF); }
        else { const int f = (n - 1 >= 2 * NW) ? n - 1 - 2 * NW : 0; fh[slot][f] = ldsr(LP(ah[ks]) + sl * HST + f * 4096); }
    };
    auto HALF = [&](int slot, bool rd, int nbuf, int nks) {
#pragma unroll
        for (int n = 0; n < 20; ++n) {
            const int m = n / 5, f = n % 5;
            if (f < NW) { const int ff = f < NW ? f : 0;
                const bf16x8 w8 = __builtin_shufflevector(fw[slot][ff][0], fw[slot][ff][1], 0, 1, 2, 3, 4, 5, 6, 7);
                if (QK) accw[m][ff] = MFMA16(w8, fx[slot][m], accw[m][ff]); else accw[m][ff] = MFMA16(fx[slot][m], w8, accw[m][ff]); }
            else { const int ff = f >= NW ? f - NW : 0; acch[m][ff] = MFMA16(fx[slot][m], fh[slot][ff], acch[m][ff]); }
            SB();
            if (rd && n < NRD) { RD(n, slot ^ 1, nbuf, nks); SB(); }
        }
        if (rd) { LGKM0(); SB(); } };
#pragma unroll
    for (int f = 0; f < NW; ++f) PIN(aw[f]);
    PIN(ax[0]); PIN(ax[1]); PIN(ah[0]); PIN(ah[1]);
#pragma unroll
    for (int m = 0; m < 4; ++m) {
#pragma unroll
        for (int f = 0; f < NW; ++f) PIN(accw[m][f]);
#pragma unroll
        for (int f = 0; f < NH; ++f) PIN(acch[m][f]);
    }
    RT(t3);
    BAR(); SB();
    RT(t4); CT(c4); PST(1);
#pragma unroll
    for (int n = 0; n < NRD; ++n) RD(n, 0, 0, 0);
    LGKM0(); SB();
    unsigned cs = 0;
#pragma unroll 1
    for (int it = 0; it < 2; ++it) {
#pragma unroll
        for (int sl = 0; sl < 4; ++sl) {
            HALF(0, true, sl, 1);
            BAR(); SB();
            HALF(1, true, (sl + 1) & 3, 0);
        }
    }
    RT(t5); CT(c5);
    PUT(0, 3, t3); PUT(0, 4, t4); PUT(0, 5, t5); PST(2);
    const int tile0 = rb * 4 + wm * 2;
    if (QK) {
        char* dst = ws + (wn ? WS_K : WS_Q) + (size_t)(4 * j + (g >> 1)) * 65536 + tile0 * 1024 + (g & 1) * 512 + ((i16 & 7) + 16 * (i16 >> 3)) * 16;
#pragma unroll
        for (int pr = 0; pr < 2; ++pr)
#pragma unroll
            for (int m = 0; m < 4; ++m)
                ST16(dst + pr * 131072 + (m >> 1) * 1024 + (m & 1) * 128, pack8(accw[m][(2 * pr) % NW], accw[m][(2 * pr + 1) % NW]));
    } else {
        char* dst = ws + WS_V + (size_t)(4 * j + 2 * wn) * 65536 + tile0 * 1024 + lane * 16;
#pragma unroll
        for (int f = 0; f < NW; ++f)
#pragma unroll
            for (int b = 0; b < 2; ++b) ST16(dst + f * 65536 + b * 1024, pack8(accw[2 * b][f], accw[2 * b + 1][f]));
    }
    {
        char* dst = ws + WS_AB + (size_t)((j >> 2) * 64 + tile0) * 32768 + ((j & 3) * 8 + (QK ? 0 : 2) + wn) * 1024 + lane * 16;
#pragma unroll
        for (int f = 0; f < NH; ++f)
#pragma unroll
            for (int b = 0; b < 2; ++b) ST16(dst + b * 32768 + f * 2048, pack8(acch[2 * b][f], acch[2 * b + 1][f]));
    }
}

__global__ __launch_bounds__(512, 2) void k1_proj(const u16* __restrict__ x, const u16* __restrict__ wqkv, const u16* __restrict__ wo,
                                                  const u16* __restrict__ hre, const u16* __restrict__ him, const u16* __restrict__ cre,
                                                  const u16* __restrict__ cim, const u16* __restrict__ gw, char* __restrict__ ws PROF_ARG) {
    extern __shared__ __attribute__((aligned(16))) char smem[];
    STAMP(0, 0); PST(0);
    const int tid = threadIdx.x, lane = tid & 63, wid = __builtin_amdgcn_readfirstlane(tid >> 6);
    const int xcd = blockIdx.x & 7, idx = blockIdx.x >> 3;
    const int rb = (xcd & 1) * 8 + (idx & 7), q4 = xcd >> 1, c4 = idx >> 3, j = q4 * 2 + (c4 >> 1);
    if (wid >= 4) {
        const u16* cm = (q4 >> 1) ? cim : cre; const u16* hm = j < 4 ? hre : him;
        const unsigned coff = (unsigned)((q4 & 1) * 1024 + idx * 32) * 4096 + (xcd & 1) * 2048;
        if (c4 & 1) k1_load<false>(x, wqkv, hm, cm, coff, gw, wo, ws, (LAS char*)smem, rb, j, lane, wid - 4);
        else        k1_load<true>(x, wqkv, hm, cm, coff, gw, wo, ws, (LAS char*)smem, rb, j, lane, wid - 4);
        STAMPW(0, 2, 256);
    } else {
        if (c4 & 1) k1_mma<false>(ws, (LAS char*)smem, rb, j, lane, wid PROF_FWD);
        else        k1_mma<true>(ws, (LAS char*)smem, rb, j, lane, wid PROF_FWD);
        STAMP(0, 1);
        wait_v<0>(); PST(3);
    }
}

#define AT_PRIO 0
#define K2_PRIO 1
constexpr int K2_NS = 3, K2_BOFF = K2_NS * 16384, K2_NKT = 16;
constexpr int K2_LK = 2 * K2_BOFF, K2_LV = K2_LK + 16384, K2_L1 = K2_LV + 16384, K2_LDS = K2_L1 + 16384;

DI void k2_gemm(const u16* __restrict__ cmat, char* __restrict__ ws, LAS char* lds, int ksl, int tp, int nt, int lane, int wid PROF_ARG) {
    const int wm = wid >> 1, wn = wid & 1, i32 = lane & 31, h = lane >> 5;
    const unsigned voffA = (lane >> 3) * 4096 + (((lane & 7) ^ (4 * (wid & 1) + (lane >> 4))) << 4);
    const unsigned voffB = lane * 16;
    const unsigned soffA = (unsigned)(tp * 128 + wid * 8) * 4096 + (ksl & 1) * 2048;
    const unsigned soffB = (unsigned)(ksl * 32 + (wid >> 1)) * 32768 + (nt * 8 + (wid & 1) * 4) * 1024;
    const char* abp = ws + WS_AB;
    auto piece = [&](int p, int kt, int sl) {
        const bool ok = kt < K2_NKT;
        if ((p & 1) == 0) glds<AUX_C>(mk_rsrc(cmat, ok ? SEQ * SEQ * 2 : 0), lds + sl * 16384 + wid * 1024 + (p >> 1) * 4096, voffA, soffA + (p >> 1) * 131072 + kt * 128);
        else              glds(mk_rsrc(abp, ok ? 4u << 20 : 0), lds + K2_BOFF + sl * 16384 + wid * 4096 + (p >> 1) * 1024, voffB, soffB + kt * 65536 + (p >> 1) * 1024);
    };
    unsigned ac[4];
#pragma unroll
    for (int ks = 0; ks < 4; ++ks) ac[ks] = LA(lds) + (wm * 64 + i32) * 128 + (((2 * ks + h) ^ ((i32 >> 1) & 7)) << 4);
    unsigned ab = LA(lds) + K2_BOFF + wn * 4096 + (i32 >> 4) * 1024 + h * 256 + (i32 & 15) * 16;
    f32x16 acc[2][2];
#pragma unroll
    for (int m = 0; m < 2; ++m)
#pragma unroll
        for (int n = 0; n < 2; ++n)
#pragma unroll
            for (int r = 0; r < 16; ++r) acc[m][n][r] = 0.f;
    bf16x8 fc[2][2], fb[2][2];
    auto RDC = [&](int slot, int m, int sl, int ks) { fc[slot][m] = ldsr(LP(ac[ks]) + sl * 16384 + m * 4096); };
    auto RDB = [&](int slot, int n, int sl, int ks) { fb[slot][n] = ldsr(LP(ab) + sl * 16384 + (ks >> 1) * 8192 + (ks & 1) * 512 + n * 2048); };
    auto QUART = [&](int slot, int nbuf, int nks, int p0, int kt2, int buf2) {
        acc[0][0] = MFMA32(fb[slot][0], fc[slot][0], acc[0][0]); SB();
        RDB(slot ^ 1, 0, nbuf, nks); RDC(slot ^ 1, 0, nbuf, nks); SB();
        acc[0][1] = MFMA32(fb[slot][1], fc[slot][0], acc[0][1]); SB();
        RDB(slot ^ 1, 1, nbuf, nks); RDC(slot ^ 1, 1, nbuf, nks); SB();
        acc[1][1] = MFMA32(fb[slot][1], fc[slot][1], acc[1][1]); SB();
        if (p0 >= 0) { piece(p0, kt2, buf2); SB(); }
        acc[1][0] = MFMA32(fb[slot][0], fc[slot][1], acc[1][0]); SB();
        if (p0 >= 0) { piece(p0 + 1, kt2, buf2); SB(); }
    };
#pragma unroll
    for (int s = 0; s < K2_NS - 1; ++s)
#pragma unroll
        for (int p = 0; p < 8; ++p) piece(p, s, s);
    piece(0, K2_NS - 1, K2_NS - 1); piece(1, K2_NS - 1, K2_NS - 1);
    __builtin_amdgcn_s_setprio(K2_PRIO);
    PIN(ac[0]); PIN(ac[1]); PIN(ac[2]); PIN(ac[3]); PIN(ab);
    PIN(acc[0][0]); PIN(acc[0][1]); PIN(acc[1][0]); PIN(acc[1][1]);
    RT(t3);
    wait_v<(K2_NS - 2) * 8 + 2>(); BAR(); SB();
    RT(t4); CT(c4);
    unsigned long long pt5 = __builtin_amdgcn_s_memrealtime(); LGKM0();
    RDB(0, 0, 0, 0); RDC(0, 0, 0, 0); RDB(0, 1, 0, 0); RDC(0, 1, 0, 0); LGKM0(); SB();
    unsigned cs = 0;
#pragma unroll 1
    for (int k0 = 0; k0 < K2_NKT - 1; k0 += 3) {
#pragma unroll
        for (int sl = 0; sl < 3; ++sl) {
            const int kt = k0 + sl;
            QUART(0, sl, 1, 2, kt + 2, (sl + 2) % 3);
            QUART(1, sl, 2, 4, kt + 2, (sl + 2) % 3);
            QUART(0, sl, 3, 6, kt + 2, (sl + 2) % 3);
            wait_v<8>(); LGKM0(); BAR(); SB();
            QUART(1, (sl + 1) % 3, 0, 0, kt + 3, sl);
        }
    }
    QUART(0, 0, 1, -1, 0, 0); QUART(1, 0, 2, -1, 0, 0); QUART(0, 0, 3, -1, 0, 0); QUART(1, 0, 0, -1, 0, 0);
    RT(t5); CT(c5);
    PUT(1, 3, t3); PUT(1, 4, t4); PUT(1, 5, t5); PST(6);
    if (blockIdx.x == PB && threadIdx.x == 0) ((volatile unsigned long long*)(ws + (30u << 20)))[5] = pt5;
    char* dst = ws + WS_P + (size_t)ksl * (2u << 20) + (size_t)(tp * 8 + wm * 4 + (i32 >> 4)) * 16384 + (nt * 4 + wn * 2) * 1024 + h * 256 + (i32 & 15) * 16;
#pragma unroll
    for (int m = 0; m < 2; ++m)
#pragma unroll
        for (int n = 0; n < 2; ++n)
#pragma unroll
            for (int b = 0; b < 2; ++b) {
                const f32x16& a = acc[m][n];
                u32x4 v; v[0] = cvtpk(a[4 * b], a[4 * b + 1]); v[1] = cvtpk(a[4 * b + 2], a[4 * b + 3]);
                v[2] = cvtpk(a[8 + 4 * b], a[9 + 4 * b]); v[3] = cvtpk(a[10 + 4 * b], a[11 + 4 * b]);
                ST16(dst + m * 32768 + n * 1024 + b * 512, v);
            }
}

#define MX3(a, b, c) __builtin_fmaxf(__builtin_fmaxf((a), (b)), (c))
#define PACE() do { SB(); BAR(); SB(); } while (0)
DI void k2_attn(char* __restrict__ ws, LAS char* lds, int head, int qb, int lane, int aw) {
    const int i32 = lane & 31, h = lane >> 5;
    {
        const unsigned vo = lane * 16;
#pragma unroll
        for (int i = 0; i < 4; ++i) {
            const int t = aw + 4 * i; const int gt = qb * 8 - 8 + t; const bool ok = gt >= 0;
            const unsigned so = (unsigned)head * 65536 + (ok ? gt : 0) * 1024;
            glds(mk_rsrc(ws + WS_K, ok ? 2u << 20 : 0), lds + K2_LK + t * 1024, vo, so);
            glds(mk_rsrc(ws + WS_V, ok ? 2u << 20 : 0), lds + K2_LV + t * 1024, vo, so);
        }
    }
    const int qt0 = qb * 8 + aw * 2;
    bf16x8 q0 = *(const bf16x8*)(ws + WS_Q + (size_t)head * 65536 + qt0 * 1024 + lane * 16);
    bf16x8 q1 = *(const bf16x8*)(ws + WS_Q + (size_t)head * 65536 + qt0 * 1024 + 1024 + lane * 16);
    if (aw == 0) {
        const u32x4 ones = {0x3f803f80u, 0x3f803f80u, 0x3f803f80u, 0x3f803f80u};
        *(LAS u32x4*)(lds + K2_L1 + i32 * 512 + h * 64) = ones;
    }
    __builtin_amdgcn_s_setprio(AT_PRIO);
    BAR();
    wait_v<0>(); WAIT_L0(); BAR();
    const int ql = (i32 & 19) | ((i32 & 4) << 1) | ((i32 & 8) >> 1);
    const int thr = ql - 8 * h;
    const unsigned kaddr = K2_LK + aw * 2048 + lane * 16;
    const unsigned vaddr = aw * 2048 + (i32 < 16 ? K2_LV + h * 256 + i32 * 16 : K2_L1 + ((i32 >= 20 && i32 < 28) ? 64 : 0));
    constexpr float C2 = 0.25f * 1.4426950408889634f;
    const float NINF = -__builtin_inff();
    f32x16 zero16;
#pragma unroll
    for (int r = 0; r < 16; ++r) zero16[r] = 0.f;
#pragma unroll 1
    for (int it = 0; it < 2; ++it) {
        const bf16x8 qf = it ? q1 : q0;
        const int qt = qt0 + it;
        LAS char* kp = lds + kaddr + it * 1024;
        LAS char* vp = lds + vaddr + it * 1024;
        f32x16 s[8];
        {
            bf16x8 kf[9];
#pragma unroll
            for (int t = 0; t < 9; ++t) kf[t] = ldsr(kp + (t == 0 ? 8 : t - 1) * 1024);
            SB();
            f32x16 s8 = MFMA32(kf[0], qf, zero16);
#pragma unroll
            for (int t = 0; t < 8; ++t) s[t] = MFMA32(kf[t + 1], qf, zero16);
            if (qt < 8) {
#pragma unroll
                for (int t = 0; t < 8; ++t) if (qt + t < 8) {
#pragma unroll
                    for (int r = 0; r < 16; ++r) s[t][r] = NINF; }
            }
#pragma unroll
            for (int r = 0; r < 16; ++r) s[0][r] = (16 * (r >> 3) + (r & 7) > thr) ? s[0][r] : s8[r];
            PIN(s[0]);
        }
        PACE();
#pragma unroll
        for (int t = 0; t < 8; ++t) PIN(s[t]);
        float m;
        {
            float ma = MX3(s[0][0], s[0][1], s[0][2]), mb = s[0][3];
#pragma unroll
            for (int e = 4; e < 128; e += 4) {
                ma = MX3(ma, s[e >> 4][e & 15], s[e >> 4][(e & 15) + 1]); mb = MX3(mb, s[e >> 4][(e & 15) + 2], s[e >> 4][(e & 15) + 3]); }
            m = __builtin_fmaxf(ma, mb);
            auto rr = __builtin_amdgcn_permlane32_swap(__float_as_uint(m), __float_as_uint(m), false, false);
            m = __builtin_fmaxf(__uint_as_float(rr[0]), __uint_as_float(rr[1]));
        }
        float nm = -m * C2;
        f32x16 o = zero16;
#pragma unroll
        for (int u = 0; u < 16; ++u) {
            const int t = u >> 1, r0 = (u & 1) * 8;
            const bf16x8 vf = ldsr(vp + t * 1024 + (u & 1) * 512);
            u32x4 p;
#pragma unroll
            for (int r = 0; r < 8; r += 2) {
                const float e0 = __builtin_amdgcn_exp2f(__builtin_fmaf(s[t][r0 + r], C2, nm)), e1 = __builtin_amdgcn_exp2f(__builtin_fmaf(s[t][r0 + r + 1], C2, nm));
                p[r >> 1] = cvtpk(e0, e1);
            }
            if (t == 0) {
                u32x4 p8;
#pragma unroll
                for (int r = 0; r < 8; r += 2) {
                    const int k0 = 2 * r0 + r;
                    const unsigned mk = (k0 > thr ? 0xffffu : 0u) | (k0 + 1 > thr ? 0xffff0000u : 0u);
                    p8[r >> 1] = p[r >> 1] & ~mk; p[r >> 1] &= mk;
                }
                o = MFMA32(ldsr(vp + 8 * 1024 + (u & 1) * 512), __builtin_bit_cast(bf16x8, p8), o);
            }
            o = MFMA32(vf, __builtin_bit_cast(bf16x8, p), o);
            if (u % 3 == 0 && u < 15) { PACE(); PIN(nm); }
        }
        PACE();
        const float rl = __builtin_amdgcn_rcpf(o[8]);
        u32x4 ov; ov[0] = cvtpk(o[0] * rl, o[1] * rl); ov[1] = cvtpk(o[2] * rl, o[3] * rl); ov[2] = cvtpk(o[4] * rl, o[5] * rl); ov[3] = cvtpk(o[6] * rl, o[7] * rl);
        ST16(ws + WS_O + (size_t)(qt * 2 + (ql >> 4)) * 16384 + (head >> 1) * 1024 + ((head & 1) * 2 + h) * 256 + (ql & 15) * 16, ov);
    }
}

__global__ __launch_bounds__(512, 2) void k2_mix(const u16* __restrict__ cre, const u16* __restrict__ cim, char* __restrict__ ws PROF_ARG) {
    extern __shared__ __attribute__((aligned(16))) char smem[];
    STAMP(1, 0); PST(4);
    const int tid = threadIdx.x, lane = tid & 63, wid = __builtin_amdgcn_readfirstlane(tid >> 6);
    const int xcd = blockIdx.x & 7, idx = blockIdx.x >> 3, q4 = xcd >> 1, rh = xcd & 1;
    if (wid < 4) {
        k2_gemm((q4 >> 1) ? cim : cre, ws, (LAS char*)smem, (q4 >> 1) * 2 + rh, (q4 & 1) * 8 + (idx >> 2), idx & 3, lane, wid PROF_FWD);
        STAMP(1, 1);
        wait_v<0>(); PST(7);
    } else {
        if (ABL != 5) k2_attn(ws, (LAS char*)smem, q4 * 8 + (idx >> 2), rh * 4 + (idx & 3), lane, wid - 4);
        STAMPW(1, 2, 256);
    }
}

constexpr int K3_LDS = 131072;
__global__ __launch_bounds__(256, 2) void k3_out(const char* __restrict__ ws, u16* __restrict__ out PROF_ARG) {
    extern __shared__ __attribute__((aligned(16))) char smem[];
    STAMP(2, 0);
    const unsigned long long p8 = __builtin_amdgcn_s_memrealtime();
    LAS char* lds = (LAS char*)smem;
    const int tid = threadIdx.x, lane = tid & 63, wid = __builtin_amdgcn_readfirstlane(tid >> 6);
    const int xcd = blockIdx.x & 7, idx = blockIdx.x >> 3;
    const int tb = xcd * 4 + (idx >> 3), eb = idx & 7;
    const int wt = wid >> 1, we = wid & 1, i16 = lane & 15, g = lane >> 4;
    const rsrc_t ro = mk_rsrc(ws + WS_O, 2u << 20), rw = mk_rsrc(ws + WS_WO, 512u << 10);
    const unsigned vo = lane * 16, so = (unsigned)(tb * 4 + wid) * 16384, sw = (unsigned)(eb * 4 + wid) * 1024;
#pragma unroll
    for (int kb = 0; kb < 16; ++kb) {
        glds(ro, lds + kb * 8192 + wid * 1024, vo, so + kb * 1024);
        glds(rw, lds + kb * 8192 + 4096 + wid * 1024, vo, sw + kb * 32768);
    }
    u32x4 pp[4][2]; float gg[2];
#pragma unroll
    for (int m = 0; m < 2; ++m) {
        const char* src = ws + WS_P + (size_t)(tb * 4 + wt * 2 + m) * 16384 + (eb * 2 + we) * 1024 + lane * 16;
#pragma unroll
        for (int k = 0; k < 4; ++k) asm volatile("global_load_dwordx4 %0, %1, off" : "=v"(pp[k][m]) : "v"(src + (size_t)k * (2u << 20)));
        asm volatile("global_load_dword %0, %1, off" : "=v"(gg[m]) : "v"((const float*)(ws + WS_G) + tb * 64 + wt * 32 + m * 16 + i16));
    }
    f32x4 acc[2][2];
#pragma unroll
    for (int m = 0; m < 2; ++m) { acc[m][0] = f32x4{0.f, 0.f, 0.f, 0.f}; acc[m][1] = f32x4{0.f, 0.f, 0.f, 0.f}; }
    LAS char* pa = lds + wt * 2048 + lane * 16;
    LAS char* pb = lds + 4096 + we * 2048 + lane * 16;
#pragma unroll
    for (int ph = 0; ph < 4; ++ph) {
        if (ph == 0) wait_v<34>(); else if (ph == 1) wait_v<26>(); else if (ph == 2) wait_v<18>(); else wait_v<10>();
        BAR(); SB();
#pragma unroll
        for (int kk = 0; kk < 4; ++kk) {
            const int kb = ph * 4 + kk;
            const bf16x8 o0 = ldsr(pa + kb * 8192), o1 = ldsr(pa + kb * 8192 + 1024), w0 = ldsr(pb + kb * 8192), w1 = ldsr(pb + kb * 8192 + 1024);
            acc[0][0] = MFMA16(w0, o0, acc[0][0]); acc[0][1] = MFMA16(w1, o0, acc[0][1]);
            acc[1][0] = MFMA16(w0, o1, acc[1][0]); acc[1][1] = MFMA16(w1, o1, acc[1][1]);
        }
        SB();
    }
    asm volatile("s_waitcnt vmcnt(0)" : "+v"(pp[0][0]), "+v"(pp[1][0]), "+v"(pp[2][0]), "+v"(pp[3][0]), "+v"(pp[0][1]), "+v"(pp[1][1]), "+v"(pp[2][1]), "+v"(pp[3][1]),
                 "+v"(gg[0]), "+v"(gg[1]) :: "memory");
#pragma unroll
    for (int m = 0; m < 2; ++m) {
        const float gt = gg[m], og = bfround(1.f - gt);
        float r[8];
#pragma unroll
        for (int e = 0; e < 8; ++e) {
            const int w = e >> 1;
            const float y1 = (e & 1) ? bfhi(pp[0][m][w]) + bfhi(pp[1][m][w]) : bflo(pp[0][m][w]) + bflo(pp[1][m][w]);
            const float y2 = (e & 1) ? bfhi(pp[2][m][w]) + bfhi(pp[3][m][w]) : bflo(pp[2][m][w]) + bflo(pp[3][m][w]);
            r[e] = gt * acc[m][e >> 2][e & 3] + og * (y1 - y2);
        }
        u32x4 v; v[0] = cvtpk(r[0], r[1]); v[1] = cvtpk(r[2], r[3]); v[2] = cvtpk(r[4], r[5]); v[3] = cvtpk(r[6], r[7]);
        *(u32x4*)(out + (size_t)(tb * 64 + wt * 32 + m * 16 + i16) * DM + eb * 64 + we * 32 + g * 8) = v;
    }
    STAMP(2, 1);
    if (blockIdx.x == PB && threadIdx.x == 0) {
        wait_v<0>();
        const unsigned long long p9 = __builtin_amdgcn_s_memrealtime();
        volatile unsigned long long* st = (volatile unsigned long long*)(ws + (30u << 20));
        unsigned long long d = st[PROBE] - st[0];
        d *= 4; if (d > 40000) d = 40000;
        while (__builtin_amdgcn_s_memrealtime() - p9 < d) __builtin_amdgcn_s_sleep(1);
    }
}

__global__ void k0_probe(char* ws) { PST(10); }
__global__ void k4_probe(char* ws) {
    if (blockIdx.x == PB && threadIdx.x == 0) {
        const unsigned long long p = __builtin_amdgcn_s_memrealtime();
        volatile unsigned long long* st = (volatile unsigned long long*)(ws + (30u << 20));
        unsigned long long d = (p - st[9]) * 4; if (d > 40000) d = 40000;
        while (__builtin_amdgcn_s_memrealtime() - p < d) __builtin_amdgcn_s_sleep(1);
    }
}
#define PROF_PASS
extern "C" void kernel_launch(void* const* d_in, const int* in_sizes, int n_in, void* d_out, int out_size, void* d_ws, size_t ws_size,
                              hipStream_t stream) {
    const u16* x = (const u16*)d_in[0]; const u16* wqkv = (const u16*)d_in[1]; const u16* wo = (const u16*)d_in[2];
    const u16* cre = (const u16*)d_in[4]; const u16* cim = (const u16*)d_in[5];
    const u16* hre = (const u16*)d_in[6]; const u16* him = (const u16*)d_in[7]; const u16* gw = (const u16*)d_in[8];
    char* ws = (char*)d_ws + (WS_END ? ((ws_size - (24u << 20)) & ~(size_t)0xfffff) : 0);
    (void)hipFuncSetAttribute((const void*)k1_proj, hipFuncAttributeMaxDynamicSharedMemorySize, K1_LDS);
    (void)hipFuncSetAttribute((const void*)k2_mix, hipFuncAttributeMaxDynamicSharedMemorySize, K2_LDS);
    (void)hipFuncSetAttribute((const void*)k3_out, hipFuncAttributeMaxDynamicSharedMemorySize, K3_LDS);
    k1_proj<<<256, 512, K1_LDS, stream>>>(x, wqkv, wo, hre, him, cre, cim, gw, ws PROF_PASS);
    k2_mix<<<256, 512, K2_LDS, stream>>>(cre, cim, ws PROF_PASS);
    k3_out<<<256, 256, K3_LDS, stream>>>(ws, (u16*)d_out PROF_PASS);
}
```

```cpp
#include <hip/hip_runtime.h>
#include <hip/hip_bf16.h>
#include <type_traits>

typedef unsigned short u16;
typedef unsigned long long u64;
using bf16x8 = __attribute__((ext_vector_type(8))) short;
using s16x4  = __attribute__((ext_vector_type(4))) short;
using f32x16 = __attribute__((ext_vector_type(16))) float;
using f32x4  = __attribute__((ext_vector_type(4))) float;
using u32x4  = __attribute__((ext_vector_type(4))) unsigned;
typedef __bf16 bf2_t __attribute__((ext_vector_type(2)));
typedef float f2_t __attribute__((ext_vector_type(2)));
#define LAS __attribute__((address_space(3)))
#define DI __device__ __forceinline__
#define MFMA32(a, b, c) __builtin_amdgcn_mfma_f32_32x32x16_bf16(a, b, c, 0, 0, 0)
#define MFMA16(a, b, c) __builtin_amdgcn_mfma_f32_16x16x32_bf16(a, b, c, 0, 0, 0)
#define SB() __builtin_amdgcn_sched_barrier(0)
#define BAR() asm volatile("s_barrier" ::: "memory")
#define PIN(x) asm volatile("" : "+v"(x))
#define PINS(x) asm volatile("" : "+s"(x))
#define LGKM0() __builtin_amdgcn_s_waitcnt(0xC07F)
#define WAIT_L0() asm volatile("s_waitcnt lgkmcnt(0)" ::: "memory")
#define WAITV(n) asm volatile("s_waitcnt vmcnt(%0)" ::"n"(n) : "memory")
#define CFENCE() __builtin_amdgcn_fence(__ATOMIC_ACQUIRE, "wavefront")
template <int I, int N, class F> DI void sfor(F&& f) { if constexpr (I < N) { f(std::integral_constant<int, I>{}); sfor<I + 1, N>(f); } }
typedef __amdgpu_buffer_rsrc_t rsrc_t;
constexpr int SC1 = 16;
DI rsrc_t mk_rsrc(const void* p, unsigned bytes) { return __builtin_amdgcn_make_buffer_rsrc((void*)p, 0, bytes, 0x00020000); }
template <int AUX = 0> DI void glds(rsrc_t r, LAS char* dst, unsigned voff, unsigned soff) {
    __builtin_amdgcn_raw_ptr_buffer_load_lds(r, (LAS void*)dst, 16, voff, soff, 0, AUX);
    SB();
}
DI unsigned cvtpk(float lo, float hi) { f2_t v = {lo, hi}; bf2_t r = __builtin_convertvector(v, bf2_t); return __builtin_bit_cast(unsigned, r); }
DI u32x4 pack8(const f32x4& a, const f32x4& b) { u32x4 v; v[0] = cvtpk(a[0], a[1]); v[1] = cvtpk(a[2], a[3]); v[2] = cvtpk(b[0], b[1]); v[3] = cvtpk(b[2], b[3]); return v; }
DI float bflo(unsigned u) { return __uint_as_float(u << 16); }
DI float bfhi(unsigned u) { return __uint_as_float(u & 0xffff0000u); }
DI float bfround(float f) { return bflo(cvtpk(f, f)); }
DI LAS char* LP(unsigned a) { return (LAS char*)(size_t)a; }
DI unsigned LA(LAS char* p) { return (unsigned)(size_t)p; }
DI bf16x8 ldsr(LAS char* p) { return *(LAS bf16x8*)p; }
DI s16x4 ldtr(LAS char* p) { return __builtin_amdgcn_ds_read_tr16_b64_v4i16((LAS s16x4*)p); }
DI bf16x8 cat8(s16x4 a, s16x4 b) { return __builtin_shufflevector(a, b, 0, 1, 2, 3, 4, 5, 6, 7); }
DI void ST16(rsrc_t r, unsigned voff, unsigned soff, u32x4 v) {
    __builtin_amdgcn_raw_buffer_store_b128(v, r, voff, soff, SC1);
    asm volatile("s_nop 1" :: "v"(v) : "memory");
}

constexpr int SEQ = 2048, DM = 512;
constexpr unsigned WS_Q = 0, WS_K = 2u << 20, WS_V = 4u << 20, WS_AB = 6u << 20, WS_O = 10u << 20, WS_P = 12u << 20, WS_G = 20u << 20,
                   WS_FB = WS_G + 65536,
                   WS_FQ = WS_FB + 8192,
                   WS_FV = WS_FQ + 8192,
                   WS_FO = WS_FV + 8192,
                   WS_FP = WS_FO + 8192,
                   WS_BYTES = 21u << 20;

typedef __attribute__((address_space(1))) u64 gu64;
extern "C" __device__ u64 hw_dispatch_id() __asm("llvm.amdgcn.dispatch.id");
DI u64 launch_tag() { return ((hw_dispatch_id() + 1) * 0x9E3779B97F4A7C15ull) ^ ((u64)__builtin_amdgcn_dispatch_ptr() * 0xD6E8FEB86659FD93ull); }
DI void flag_set(char* ws, unsigned off, u64 tag) { __hip_atomic_store((gu64*)(ws + off), tag, __ATOMIC_RELAXED, __HIP_MEMORY_SCOPE_AGENT); }
DI u64 flag_get(const char* ws, unsigned off) { return __hip_atomic_load((gu64*)(ws + off), __ATOMIC_RELAXED, __HIP_MEMORY_SCOPE_AGENT); }
DI void flag_wait(const char* ws, unsigned off, u64 tag) {
#pragma unroll 1
    for (int spin = 0; spin < (1 << 20); ++spin) {
        if (__all(flag_get(ws, off) == tag)) break;
        __builtin_amdgcn_s_sleep(2);
    }
}
DI bool stores_out(unsigned n = 0) { return (__builtin_amdgcn_s_getreg((3 << 11) | 7) & 15) <= n; }
#define TRY_PUBLISH(done, force, off) do { if (!(done) && ((force) || stores_out())) { WAITV(0); if (lane == 0) flag_set(ws, off, tag); done = true; } } while (0)
DI bool lds_full(LAS char* p) {
    unsigned c; asm volatile("ds_read_b32 %0, %1\n\ts_waitcnt lgkmcnt(0)" : "=v"(c) : "v"(LA(p)) : "memory");
    return __builtin_amdgcn_readfirstlane(c) == 8;
}
#define FLAG_ISSUE(f, p) asm volatile("global_load_dwordx2 %0, %1, off sc1" : "=v"(f) : "v"(p))
#define FLAG_LANDED(f, n) asm volatile("s_waitcnt vmcnt(%1)" : "+v"(f) : "n"(n) : "memory")

constexpr int LDS_BYTES = 163840, P1_RING = 81920, L_Q = 65536, L_GW = L_Q + 8192, L_DUMMY = L_GW + 1024, L_CNT = L_DUMMY + 1024;
constexpr int L_K = 0, L_V = 16384, VST = 1104, L_DUMMY2 = 36864, L_AB = 32768, L_C = 98304;
DI constexpr int ab_slot(int k) { return ((k + 1) & 3) * 16384; }
DI constexpr int p3_o(int c) { return c == 0 ? 0 : L_C + (c - 1) * 16384; }
DI constexpr int p3_w(int c) { return c == 0 ? 16384 : L_AB + ab_slot(c - 1); }
constexpr int CODE_BYTES = 32000;
constexpr float QSCALE = 0.25f * 1.4426950408889634f;

DI float mx3(float a, float b, float c) { return __builtin_elementwise_maximum(__builtin_elementwise_maximum(a, b), c); }
DI float max16(float m, const f32x16& s) {
    float a = mx3(m, s[0], s[1]), b = mx3(s[2], s[3], s[4]);
    a = mx3(a, s[5], s[6]); b = mx3(b, s[7], s[8]); a = mx3(a, s[9], s[10]); b = mx3(b, s[11], s[12]); a = mx3(a, s[13], s[14]);
    return mx3(a, b, s[15]);
}
constexpr int lane_query(int i32) { return (i32 & 19) | ((i32 & 4) << 1) | ((i32 & 8) >> 1); }
constexpr u64 edge_mask(int r) {
    u64 m = 0;
    for (int l = 0; l < 64; ++l) if (16 * (r >> 3) + 8 * (l >> 5) + (r & 7) > lane_query(l & 31)) m |= 1ull << l;
    return m;
}
struct AttnLane { u32x4 mk[2]; unsigned kp, vp0, vp1, ovo; };
DI void attn_setup(AttnLane& a, LAS char* lds, int lane, int wid) {
    const int i32 = lane & 31, h = lane >> 5, ql = lane_query(i32), thr = ql - 8 * h;
#pragma unroll
    for (int r = 0; r < 8; ++r) { const int k0 = 16 * (r >> 2) + 2 * (r & 3); a.mk[r >> 2][r & 3] = (k0 > thr ? 0xffffu : 0u) | (k0 + 1 > thr ? 0xffff0000u : 0u); }
    const unsigned vpad = 1024 + ((i32 >= 20 && i32 < 28) ? 64 : 0);
    a.kp = LA(lds) + L_K + wid * 1024 + lane * 16;
    a.vp0 = LA(lds) + L_V + wid * VST + (i32 < 16 ? h * 256 + i32 * 16 : vpad);
    a.vp1 = LA(lds) + L_V + wid * VST + (i32 < 16 ? 512 + h * 256 + i32 * 16 : vpad);
    a.ovo = (ql >> 4) * 16384 + h * 256 + (ql & 15) * 16;
    PIN(a.mk[0]); PIN(a.mk[1]); PIN(a.kp); PIN(a.vp0); PIN(a.vp1); PIN(a.ovo);
}
template <int R> DI float edge_sel(float a, float b) { constexpr u64 m = edge_mask(R); return __builtin_amdgcn_inverse_ballot_w64(m) ? a : b; }
template <class F>
DI void attention(const AttnLane& al, rsrc_t rs, int head, int qt, F&& hook) {
    LAS char* kp = LP(al.kp); LAS char* vp0 = LP(al.vp0); LAS char* vp1 = LP(al.vp1);
    f32x16 zero16;
#pragma unroll
    for (int r = 0; r < 16; ++r) zero16[r] = 0.f;
    const bf16x8 qf = ldsr(LP(al.kp + (L_Q - L_K)));
    bf16x8 kf[9];
#pragma unroll
    for (int t = 8; t >= 0; --t) kf[(t + 1) % 9] = ldsr(kp + ((t + 1) % 9) * 1024);
    f32x16 sm, s[2];
    sm = MFMA32(kf[0], qf, zero16);
    s[0] = MFMA32(kf[8], qf, zero16);
    s[1] = MFMA32(kf[7], qf, zero16);
    hook(0);
    if (__builtin_expect(qt < 8, 0)) {
        float ni = -__builtin_inff(); PIN(ni);
#pragma unroll
        for (int r = 0; r < 16; ++r) sm[r] = ni;
    }
#define ES(r) sm[r] = edge_sel<r>(sm[r], s[0][r])
    ES(0); ES(1); ES(2); ES(3); ES(4); ES(5); ES(6); ES(7); ES(8); ES(9); ES(10); ES(11); ES(12); ES(13); ES(14); ES(15);
#undef ES
    s[0] = MFMA32(kf[6], qf, zero16);
    float ma = max16(sm[0], sm);
    SB();
#pragma unroll
    for (int t = 7; t >= 1; --t) {
        const float mt = max16(ma, s[t & 1]);
        ma = (qt + t < 8) ? ma : mt;
        if (t > 2) s[t & 1] = MFMA32(kf[t - 2], qf, zero16);
        SB();
    }
    const auto rr = __builtin_amdgcn_permlane32_swap(__float_as_uint(ma), __float_as_uint(ma), false, false);
    float nm = -__builtin_elementwise_maximum(__uint_as_float(rr[0]), __uint_as_float(rr[1]));
    PIN(nm);
    f32x16 cv;
#pragma unroll
    for (int r = 0; r < 16; ++r) cv[r] = nm;
    PIN(cv);
    sm += cv;
    auto CUT = [&](int t) {
        if (__builtin_expect(qt + t == 7, 0)) {
            float ni = -__builtin_inff(); PIN(ni);
#pragma unroll
            for (int r = 0; r < 16; ++r) cv[r] = ni;
            PIN(cv);
        } };
    PIN(sm); CUT(7);
    s[1] = MFMA32(kf[7], qf, cv);
#pragma unroll
    for (int r = 0; r < 16; ++r) sm[r] = __builtin_amdgcn_exp2f(sm[r]);
    PIN(sm);
    hook(1);
    f32x16 o = zero16;
    {
        const bf16x8 va0 = ldsr(vp0), va1 = ldsr(vp1), vb0 = ldsr(vp0 + 8 * VST), vb1 = ldsr(vp1 + 8 * VST);
        u32x4 p[2];
#pragma unroll
        for (int r = 0; r < 8; ++r) p[r >> 2][r & 3] = cvtpk(sm[2 * r], sm[2 * r + 1]);
        o = MFMA32(va0, __builtin_bit_cast(bf16x8, p[0] & al.mk[0]), o);  o = MFMA32(va1, __builtin_bit_cast(bf16x8, p[1] & al.mk[1]), o);
        o = MFMA32(vb0, __builtin_bit_cast(bf16x8, p[0] & ~al.mk[0]), o); o = MFMA32(vb1, __builtin_bit_cast(bf16x8, p[1] & ~al.mk[1]), o);
        SB();
    }
#pragma unroll
    for (int t = 7; t >= 1; --t) {
        hook(9 - t);
        if (t > 1) CUT(t - 1);
        const bf16x8 v0 = ldsr(vp0 + t * VST), v1 = ldsr(vp1 + t * VST);
        f32x16& e = s[t & 1];
        u32x4 p0, p1;
#pragma unroll
        for (int r = 0; r < 5; ++r) e[r] = __builtin_amdgcn_exp2f(e[r]);
        PIN(e); SB();
        if (t > 1) s[(t - 1) & 1] = MFMA32(kf[t - 1], qf, cv);
        SB();
#pragma unroll
        for (int r = 5; r < 10; ++r) e[r] = __builtin_amdgcn_exp2f(e[r]);
        PIN(e); SB();
#pragma unroll
        for (int r = 0; r < 4; ++r) p0[r] = cvtpk(e[2 * r], e[2 * r + 1]);
        e[10] = __builtin_amdgcn_exp2f(e[10]);
        PIN(e); PIN(p0); SB();
        o = MFMA32(v0, __builtin_bit_cast(bf16x8, p0), o);
        SB();
#pragma unroll
        for (int r = 11; r < 16; ++r) e[r] = __builtin_amdgcn_exp2f(e[r]);
        PIN(e); SB();
#pragma unroll
        for (int r = 0; r < 4; ++r) p1[r] = cvtpk(e[8 + 2 * r], e[9 + 2 * r]);
        o = MFMA32(v1, __builtin_bit_cast(bf16x8, p1), o);
        SB();
    }
    const float rl = __builtin_amdgcn_rcpf(o[8]);
    u32x4 ov;
#pragma unroll
    for (int r = 0; r < 4; ++r) ov[r] = cvtpk(o[2 * r] * rl, o[2 * r + 1] * rl);
    ST16(rs, al.ovo, WS_O + (unsigned)qt * 32768 + (head >> 1) * 1024 + (head & 1) * 512, ov);
}

__global__ __launch_bounds__(512, 2) void gated_mix_fused(const u16* x, const u16* wqkv, const u16* wo, const u16* cre, const u16* cim,
                                                          const u16* hre, const u16* him, const u16* gw, char* ws, u16* out) {
    extern __shared__ __attribute__((aligned(16))) char smem[];
    LAS char* lds = (LAS char*)smem;
    u64 code_anchor; asm volatile("s_getpc_b64 %0" : "=s"(code_anchor));
    const int tid = threadIdx.x, lane = tid & 63, wid8 = __builtin_amdgcn_readfirstlane(tid >> 6), wid = wid8 & 3;
    const bool mma = wid8 < 4;
    const int xcd = blockIdx.x & 7, idx = blockIdx.x >> 3, q4 = xcd >> 1, rh = xcd & 1;
    const int rb = rh * 16 + (idx & 15), j = q4 * 2 + (idx >> 4);
    const int ksl = (q4 >> 1) * 2 + rh, tp = (q4 & 1) * 8 + (idx >> 2), nt = idx & 3;
    const int head = q4 * 8 + (idx >> 2), qb = rh * 4 + (idx & 3);
    const int tb = tp * 2 + (ksl >> 1), eb = nt * 2 + (ksl & 1);
    const int i16 = lane & 15, g = lane >> 4, wm = wid >> 1, wn = wid & 1;
    if (idx < 4 && wid8 == 0) {
        const int co = idx * 8192 + lane * 128;
        __builtin_amdgcn_global_load_lds((const __attribute__((address_space(1))) void*)(code_anchor + (co < CODE_BYTES ? co : 0)), (LAS void*)(lds + L_DUMMY), 4, 0, 0);
    }
    SB();
    unsigned voffX = (lane >> 3) * 1024 + (((lane & 7) ^ (((lane >> 4) & 1) | (wid << 1))) << 4);
    unsigned voffW, voffV;
    { const int r = lane >> 4, v = r | (((wid >> 1) & 1) << 2), sw = (v & 1) | ((v >> 1) << 2), c = (lane & 15) ^ sw;
      voffW = r * 3072 + (c >> 3) * 1024 + (c & 7) * 16; }
    { const int r = lane >> 3, u = ((r >> 1) & 1) | ((wid & 1) << 1), c = (lane & 7) ^ (u << 1);
      voffV = r * 3072 + c * 16; }
    unsigned soffX = (unsigned)(rb * 64 + wid * 8) * 1024, soffH = (unsigned)((j & 3) * 128 + wid * 8) * 1024;
    unsigned soffW = (unsigned)(wid * 4) * 3072 + j * 128, soffV = (unsigned)(wid * 8) * 3072 + 2048 + j * 128;
    int rot = (rb + 4 * j) & 7;
    u64 tag = launch_tag();
    unsigned ldsw = LA(lds) + wid * 1024, voffG = (((lane >> 3) + rot) & 7) * 128 + (lane & 7) * 16;
    PIN(voffX); PIN(voffW); PIN(voffG); PINS(soffX); PINS(soffW); PINS(rot); PINS(tag); PINS(ldsw);
    asm volatile("" : "+s"(x), "+s"(wqkv), "+s"(wo), "+s"(cre), "+s"(cim), "+s"(hre), "+s"(him), "+s"(gw), "+s"(ws), "+s"(out));
    const rsrc_t rs = mk_rsrc(ws, WS_BYTES);

    const rsrc_t rc = mk_rsrc((q4 >> 1) ? cim : cre, SEQ * SEQ * 2);
    const unsigned voffA = (lane >> 3) * 4096 + (((lane & 7) ^ (4 * (wid & 1) + (lane >> 4))) << 4);
    const unsigned soffA = (unsigned)(tp * 128 + wid * 8) * 4096 + rh * 2048;
    const unsigned soffB = WS_AB + (unsigned)(ksl * 32 + (wid >> 1)) * 32768 + (nt * 8 + (wid & 1) * 4) * 1024;
    const int roty = ((0x0B60 >> (4 * nt)) & 15) + (tp & 7);
    auto cst = [&](int kt) {
        int rr = roty; PINS(rr);
        const unsigned o = soffA + ((kt + rr) & 15) * 128;
#pragma unroll
        for (int p = 0; p < 4; ++p) glds(rc, lds + L_C + (kt & 3) * 16384 + wid * 1024 + p * 4096, voffA, o + p * 131072); };
    auto bst = [&](int kt) {
        int rr = roty; PINS(rr);
        const unsigned o = soffB + ((kt + rr) & 15) * 65536;
#pragma unroll
        for (int p = 0; p < 4; ++p) glds<SC1>(rs, lds + L_AB + ab_slot(kt) + wid * 4096 + p * 1024, lane * 16, o + p * 1024); };
    const unsigned fb_off = WS_FB + (unsigned)(((ksl >> 1) * 4 + nt) * 32 + rh * 16) * 32 + lane * 8;

    AttnLane al;
    if (mma) {
        attn_setup(al, lds, lane, wid8);
        __builtin_amdgcn_s_setprio(1);
        unsigned ax[2], axv[2], axg[2], ah[2][2], aw[2][2], av[2][2], ag;
#pragma unroll
        for (int ks = 0; ks < 2; ++ks) {
            const unsigned a = LA(lds) + (8 * (i16 >> 2) + (i16 & 3)) * 128 + (((4 * ks + g) ^ (i16 >> 1)) << 4);
            ax[ks] = a; axv[ks] = a + wm * 4096; axg[ks] = axv[ks] + wn * 512; ah[ks][0] = a + P1_RING + wid * 4096; }
        { const int q = i16 >> 2, p = lane & 3, v = q | ((g & 1) << 2), sw = (v & 1) | ((v >> 1) << 2), u = (q >> 1) | ((g & 1) << 1);
#pragma unroll
          for (int f = 0; f < 2; ++f) {
              aw[f][0] = LA(lds) + P1_RING + (8 * g + q) * 256 + (((4 * wid + f + 2 * (p >> 1)) ^ sw) << 4) + 8 * (p & 1);
              av[f][0] = LA(lds) + P1_RING + (8 * g + q) * 128 + (((4 * wn + 2 * f + (p >> 1)) ^ (u << 1)) << 4) + 8 * (p & 1); } }
        ag = LA(lds) + L_GW + g * 16; PIN(ag);
#pragma unroll
        for (int c = 0; c < 2; ++c) {
            ah[c][1] = ah[c][0] + 65536; aw[c][1] = aw[c][0] + 65536; av[c][1] = av[c][0] + 65536;
            PIN(ax[c]); PIN(axv[c]); PIN(axg[c]); PIN(ah[c][0]); PIN(ah[c][1]); PIN(aw[c][0]); PIN(aw[c][1]); PIN(av[c][0]); PIN(av[c][1]); }
        f32x4 acc[4][2], acv[2][2], acb[4][2], acg = {0.f, 0.f, 0.f, 0.f};
#pragma unroll
        for (int m = 0; m < 4; ++m) {
#pragma unroll
            for (int f = 0; f < 2; ++f) { acc[m][f] = f32x4{0.f, 0.f, 0.f, 0.f}; PIN(acc[m][f]); acb[m][f] = f32x4{0.f, 0.f, 0.f, 0.f}; PIN(acb[m][f]); }
            acv[m >> 1][m & 1] = f32x4{0.f, 0.f, 0.f, 0.f}; PIN(acv[m >> 1][m & 1]);
        }
        PIN(acg);
        bf16x8 fa[2][16];
        auto RD = [&](int n, int slot, int s) {
            const int hi = (s % 5) >> 2, so = ((s % 5) & 3) * 16384;
            if (s < 8) {
                const int ks = n >> 3, q = n & 7;
                if (q == 6) fa[slot][n] = ldsr(LP(ag) + s * 128 + ks * 64);
                else if (q == 7) fa[slot][n] = ldsr(LP(axg[ks]) + s * 8192);
                else if (q == 1 || q == 2) { LAS char* p = LP(aw[q - 1][hi]) + so + ks * 8192; fa[slot][n] = cat8(ldtr(p), ldtr(p + 1024)); }
                else { const int m = q == 0 ? 0 : q - 2; fa[slot][n] = ldsr(LP(ax[ks]) + s * 8192 + (m >> 1) * 4096 + (m & 1) * 512); }
            } else if (s < 12) {
                const int kq = n >> 2, q = n & 3;
                if (q == 1 || q == 2) { LAS char* p = LP(av[q - 1][hi]) + so + kq * 4096; fa[slot][n] = cat8(ldtr(p), ldtr(p + 512)); }
                else fa[slot][n] = ldsr(LP(axv[kq & 1]) + ((s - 8) * 2 + (kq >> 1)) * 8192 + (q == 3 ? 512 : 0));
            } else {
                const int ks = n / 6, q = n % 6, c = s - 12;
                if (q == 1 || q == 2) fa[slot][n] = ldsr(LP(ah[ks][hi]) + so + (q - 1) * 512);
                else { const int m = q == 0 ? 0 : q - 2; fa[slot][n] = ldsr(LP(ax[ks]) + c * 8192 + (m >> 1) * 4096 + (m & 1) * 512); }
            } };
        BAR(); SB();
#pragma unroll
        for (int n = 0; n < 16; ++n) RD(n, 0, 0);
        LGKM0(); SB();
        bool fq_done = false, fv_done = false;
        sfor<0, 20>([&](auto S) {
            constexpr int s = S, slot = s & 1, nm = s < 8 ? 18 : 16, nf = s == 19 ? 0 : s < 11 ? 16 : 12;
            if (s <= 16) { BAR(); SB(); }
#pragma unroll
            for (int n = 0; n < 18; ++n) if (n < nm) {
                if (s < 8) { const int ks = n / 9, q = n % 9;
                    if (q < 8) { const int m = q >> 1; acc[m][q & 1] = MFMA16(fa[slot][ks * 8 + 1 + (q & 1)], fa[slot][ks * 8 + (m == 0 ? 0 : m + 2)], acc[m][q & 1]); }
                    else acg = MFMA16(fa[slot][ks * 8 + 7], fa[slot][ks * 8 + 6], acg); }
                else if (s < 12) { const int kq = n >> 2, mm = (n >> 1) & 1; acv[mm][n & 1] = MFMA16(fa[slot][kq * 4 + mm * 3], fa[slot][kq * 4 + 1 + (n & 1)], acv[mm][n & 1]); }
                else { const int ks = n >> 3, m = (n >> 1) & 3; acb[m][n & 1] = MFMA16(fa[slot][ks * 6 + (m == 0 ? 0 : m + 2)], fa[slot][ks * 6 + 1 + (n & 1)], acb[m][n & 1]); }
                SB();
                if (nf == 12) { if (n < 2) { RD(2 * n, slot ^ 1, s + 1); RD(2 * n + 1, slot ^ 1, s + 1); } else if (n < 10) RD(n + 2, slot ^ 1, s + 1); }
                else if (nf == 16 && n < 12) { RD(n < 4 ? 2 * n : n + 4, slot ^ 1, s + 1); if (n < 4) RD(2 * n + 1, slot ^ 1, s + 1); }
                SB();
            }
            LGKM0(); SB();
            if (s == 7) {
                const unsigned vo = (g >> 1) * 65536 + (g & 1) * 512 + ((i16 & 7) + 16 * (i16 >> 3)) * 16;
                const unsigned so = ((wid >> 1) ? WS_K : WS_Q) + (unsigned)(4 * j + 2 * (wid & 1)) * 65536 + rb * 2048;
                const float sc = (wid >> 1) ? 1.f : QSCALE;
#pragma unroll
                for (int m = 0; m < 4; ++m) ST16(rs, vo, so + (m >> 1) * 1024 + (m & 1) * 128, pack8(acc[m][0] * sc, acc[m][1] * sc));
                SB();
            }
            if (s >= 10 && s <= 11) { TRY_PUBLISH(fq_done, false, WS_FQ + ((j * 32 + rb) * 4 + wid) * 8); SB(); }
            if (s == 11) {
#pragma unroll
                for (int f = 0; f < 2; ++f) ST16(rs, lane * 16, WS_V + (unsigned)(4 * j + 2 * wn + f) * 65536 + rb * 2048 + wm * 1024, pack8(acv[0][f], acv[1][f]));
                SB();
            }
            if (s >= 12) {
                if (s <= 13 && !fq_done && (s == 13 || stores_out(2))) { WAITV(2); if (lane == 0) flag_set(ws, WS_FQ + ((j * 32 + rb) * 4 + wid) * 8, tag); fq_done = true; }
                if (s >= 14 && s <= 16) TRY_PUBLISH(fv_done, s == 16, WS_FV + ((j * 32 + rb) * 4 + wid) * 8);
                SB();
            }
        });
        asm volatile("ds_add_u32 %0, %1" :: "v"(LA(lds) + L_CNT), "v"(lane == 0 ? 1u : 0u) : "memory");
        u32x4 abv[4];
#pragma unroll
        for (int n = 0; n < 4; ++n) { abv[n] = pack8(acb[2 * (n & 1)][n >> 1], acb[2 * (n & 1) + 1][n >> 1]); PIN(abv[n]); }
        auto ab_store = [&]() {
            const unsigned so = WS_AB + (unsigned)((j >> 2) * 64 + rb * 2) * 32768 + ((j & 3) * 8 + wid * 2) * 1024;
#pragma unroll
            for (int n = 0; n < 4; ++n) ST16(rs, lane * 16, so + (n & 1) * 32768 + (n >> 1) * 1024, abv[n]);
            if (j < 4) {
                u32x4 gv;
#pragma unroll
                for (int r = 0; r < 4; ++r) gv[r] = __float_as_uint(bfround(__builtin_amdgcn_rcpf(1.f + __expf(-bfround(acg[r])))));
                if (i16 == 0) ST16(rs, g * 32, WS_G + (unsigned)(j * 2048 + rb * 64 + wm * 32 + wn * 4) * 4, gv);
            } };
        __builtin_amdgcn_s_setprio(0);
        bool fb_done = false;
        ab_store();
#pragma unroll 1
        for (int spin = 0; spin < (1 << 22); ++spin) {
            TRY_PUBLISH(fb_done, false, WS_FB + ((j * 32 + rb) * 4 + wid) * 8);
            if (lds_full(lds + L_CNT)) break;
            __builtin_amdgcn_s_sleep(1);
        }
        asm volatile("" ::: "memory");
        attention(al, rs, head, qb * 8 + wid8, [&](int i) {
            if (i <= 3) TRY_PUBLISH(fb_done, i == 3, WS_FB + ((j * 32 + rb) * 4 + wid) * 8);
            if (i == 1) BAR();
        });
        {
        f32x4 yacc[4][4]; unsigned ac[2][2], ab[2];
        bf16x8 fc[2][4], fb4[2][4];
#pragma unroll
        for (int hh = 0; hh < 2; ++hh) {
#pragma unroll
            for (int ks = 0; ks < 2; ++ks) {
                ac[hh][ks] = LA(lds) + L_C + (((ksl >> 1) ^ hh) * 64 + wm * 32 + i16) * 128 + (((4 * ks + g) ^ (i16 >> 1)) << 4); PIN(ac[hh][ks]); }
            ab[hh] = LA(lds) + L_AB + ((ksl & 1) ^ hh) * 4096 + wn * 2048 + lane * 16; PIN(ab[hh]);
        }
#pragma unroll
        for (int n = 0; n < 16; ++n) { yacc[n >> 2][n & 3] = f32x4{0.f, 0.f, 0.f, 0.f}; PIN(yacc[n >> 2][n & 3]); }
        auto YRD = [&](int n, int slot, int sl, int ks) {
            if (n >= 1 && n <= 4) { const int m = n - 1; fc[slot][m] = ldsr(LP(ac[m >> 1][ks]) + sl * 16384 + (m & 1) * 2048); }
            else { const int f = n == 0 ? 0 : n - 4; fb4[slot][f] = ldsr(LP(ab[f >> 1]) + ab_slot(sl) + ks * 8192 + (f & 1) * 1024); } };
        auto YHALF = [&](int slot, int nsl, int nks) {
#pragma unroll
            for (int n = 0; n < 16; ++n) {
                const int f = n >> 2, m = (f & 1) ? 3 - (n & 3) : n & 3;
                yacc[m][f] = MFMA16(fb4[slot][f], fc[slot][m], yacc[m][f]); SB();
                if (n < 8) { YRD(n, slot ^ 1, nsl, nks); SB(); }
            }
            LGKM0(); SB(); };
        __builtin_amdgcn_s_setprio(1);
        BAR(); SB();
#pragma unroll
        for (int n = 0; n < 8; ++n) YRD(n, 0, 0, 0);
        LGKM0(); SB();
#pragma unroll 1
        for (int it = 0; it < 4; ++it) {
#pragma unroll
            for (int sl = 0; sl < 4; ++sl) {
                YHALF(0, sl, 1);
                if (sl == 2) WAITV(0);
                BAR(); SB();
                YHALF(1, (sl + 1) & 3, 0);
            }
        }
        const int peer0 = idx * 8 + (q4 & 1) * 2;
#pragma unroll
        for (int d = 1; d < 4; ++d) {
            const int k = ksl ^ d;
            const unsigned so = WS_P + (unsigned)(((peer0 + (k >> 1) * 4 + (k & 1)) * 4 + ksl) * 4 + wid) * 2048;
#pragma unroll
            for (int m = 0; m < 2; ++m) ST16(rs, lane * 16, so + m * 1024, pack8(yacc[(d >> 1) * 2 + m][(d & 1) * 2], yacc[(d >> 1) * 2 + m][(d & 1) * 2 + 1]));
        }
        const int kf = ksl ^ ((lane & 3) == 0 ? 1 : (lane & 3));
        const unsigned fp_out = WS_FP + (unsigned)(peer0 + (kf >> 1) * 4 + (kf & 1)) * 128 + wid * 32 + ksl * 8;
        const unsigned fp_in = WS_FP + blockIdx.x * 128 + wid * 32 + kf * 8;
        bool fp_done = false;
        f32x4 acc[2][2];
#pragma unroll
        for (int m = 0; m < 2; ++m) { acc[m][0] = f32x4{0.f, 0.f, 0.f, 0.f}; acc[m][1] = f32x4{0.f, 0.f, 0.f, 0.f}; }
        const unsigned pa = LA(lds) + wm * 2048 + lane * 16;
        unsigned pb;
        { const int q = i16 >> 2, p = lane & 3; pb = LA(lds) + ((g >> 1) * 16 + (g & 1) * 4 + q) * 128 + (((4 * wn + p) ^ ((q >> 1) << 2)) << 4); }
        bf16x8 po[2][2]; s16x4 pw[2][4];
        auto PRD = [&](int slot, int k) {
            LAS char* a = LP(pa + p3_o(k >> 2)) + (k & 3) * 4096; LAS char* b = LP(pb + p3_w(k >> 2)) + (k & 3) * 4096;
            po[slot][0] = ldsr(a); pw[slot][0] = ldtr(b); pw[slot][1] = ldtr(b + 1024);
            pw[slot][2] = ldtr(b + 8); pw[slot][3] = ldtr(b + 1024 + 8); po[slot][1] = ldsr(a + 1024); };
        bf16x8 po2[2][2]; s16x4 pw2[2][4];
        auto PRD2 = [&](int slot, int k) {
            LAS char* a = LP(pa + p3_o(k >> 2)) + (k & 3) * 4096; LAS char* b = LP(pb + p3_w(k >> 2)) + (k & 3) * 4096;
            po2[slot][0] = ldsr(a); pw2[slot][0] = ldtr(b); pw2[slot][1] = ldtr(b + 1024);
            pw2[slot][2] = ldtr(b + 8); pw2[slot][3] = ldtr(b + 1024 + 8); po2[slot][1] = ldsr(a + 1024); };
        PRD(0, 0); PRD2(0, 1); SB();
#pragma unroll
        for (int k = 0; k < 16; k += 2) {
            const int sl = (k >> 1) & 1;
            if (k == 6 || k == 10) { BAR(); SB(); }
            if (k + 2 < 16) { PRD(sl ^ 1, k + 2); PRD2(sl ^ 1, k + 3); }
            SB();
            { const bf16x8 w0 = cat8(pw[sl][0], pw[sl][1]), w1 = cat8(pw[sl][2], pw[sl][3]);
              acc[0][0] = MFMA16(w0, po[sl][0], acc[0][0]); acc[0][1] = MFMA16(w1, po[sl][0], acc[0][1]);
              acc[1][0] = MFMA16(w0, po[sl][1], acc[1][0]); acc[1][1] = MFMA16(w1, po[sl][1], acc[1][1]); }
            { const bf16x8 w0 = cat8(pw2[sl][0], pw2[sl][1]), w1 = cat8(pw2[sl][2], pw2[sl][3]);
              acc[0][0] = MFMA16(w0, po2[sl][0], acc[0][0]); acc[0][1] = MFMA16(w1, po2[sl][0], acc[0][1]);
              acc[1][0] = MFMA16(w0, po2[sl][1], acc[1][0]); acc[1][1] = MFMA16(w1, po2[sl][1], acc[1][1]); }
            SB();
            if (k >= 4 && !fp_done && (k == 14 || stores_out())) { WAITV(0); if (lane >= 1 && lane < 4) flag_set(ws, fp_out, tag); fp_done = true; }
        }
        flag_wait(ws, fp_in, tag);
        CFENCE();
        u32x4 pp[3][2]; float gg[2];
#pragma unroll
        for (int m = 0; m < 2; ++m) {
#pragma unroll
            for (int d = 1; d < 4; ++d)
                pp[d - 1][m] = __builtin_amdgcn_raw_buffer_load_b128(rs, lane * 16, WS_P + (unsigned)((blockIdx.x * 4 + (ksl ^ d)) * 4 + wid) * 2048 + m * 1024, SC1);
            gg[m] = __uint_as_float(__builtin_amdgcn_raw_buffer_load_b32(rs, i16 * 4, WS_G + (nt * 2048 + tb * 64 + wm * 32 + m * 16) * 4, SC1));
        }
#pragma unroll
        for (int m = 0; m < 2; ++m) {
            const float gt = gg[m], og = (ksl >> 1) ? -bfround(1.f - gt) : bfround(1.f - gt);
            float r[8];
#pragma unroll
            for (int e = 0; e < 8; ++e) {
                const int w = e >> 1;
                const float y1 = yacc[m][e >> 2][e & 3] + ((e & 1) ? bfhi(pp[0][m][w]) : bflo(pp[0][m][w]));
                const float y2 = (e & 1) ? bfhi(pp[1][m][w]) + bfhi(pp[2][m][w]) : bflo(pp[1][m][w]) + bflo(pp[2][m][w]);
                r[e] = gt * acc[m][e >> 2][e & 3] + og * (y1 - y2);
            }
            u32x4 v; v[0] = cvtpk(r[0], r[1]); v[1] = cvtpk(r[2], r[3]); v[2] = cvtpk(r[4], r[5]); v[3] = cvtpk(r[6], r[7]);
            __builtin_amdgcn_raw_buffer_store_b128(v, mk_rsrc(out, SEQ * DM * 2), (unsigned)(tb * 64 + wm * 32 + m * 16 + i16) * (DM * 2) + eb * 128 + wn * 64 + g * 16, 0, SC1);
        }
        }
    } else {
        const rsrc_t rx = mk_rsrc(x, SEQ * DM * 2), rw = mk_rsrc(wqkv, DM * 3 * DM * 2), rhm = mk_rsrc(j < 4 ? hre : him, DM * DM * 2);
        auto stage = [&](int s) {
            LAS char* b = LP(ldsw); LAS char* r = b + P1_RING + (s % 5) * 16384;
            int rr = rot; PINS(rr);
            if (s < 8) {
                const unsigned kt = (s + rr) & 7;
#pragma unroll
                for (int p = 0; p < 2; ++p) glds(rx, b + s * 8192 + p * 4096, voffX, soffX + p * 32768 + kt * 128);
#pragma unroll
                for (int p = 0; p < 4; ++p) glds(rw, r + p * 4096, voffW, soffW + p * 16 * 3072 + kt * (64 * 3072));
            } else if (s < 12) {
#pragma unroll
                for (int p = 0; p < 4; ++p) glds(rw, r + p * 4096, voffV, soffV + (p & 1) * 32 * 3072 + (((s - 8) * 2 + (p >> 1) + rr) & 7) * (64 * 3072));
            } else {
#pragma unroll
                for (int p = 0; p < 4; ++p) glds(rhm, r + p * 4096, voffX, soffH + p * 32768 + ((s - 12 + rr) & 7) * 128);
            } };
        auto np = [](int s) { return s > 19 ? 0 : s < 8 ? 6 : 4; };
        stage(0);
        glds(mk_rsrc(gw, DM * 2), lds + L_GW, voffG, 0);
        stage(1); stage(2); stage(3);
        WAITV(18); BAR();
        stage(4);
#pragma unroll
        for (int s = 0; s < 13; ++s) {
            WAITV(np(s + 2) + np(s + 3) + np(s + 4)); BAR();
            stage(s + 5);
            if (s == 5 && tid == 256) { *(volatile LAS u64*)(lds + L_CNT) = 0; WAIT_L0(); }
        }
        int prb = qb * 4 - 4 + ((lane & 31) >> 2); if (prb < 0) prb = 0;
        const unsigned fq_off = WS_FQ + (unsigned)(((head >> 2) * 32 + prb) * 4 + ((head >> 1) & 1) + (lane & 2)) * 8;
        const unsigned fv_off = fq_off + (WS_FV - WS_FQ);
        auto kvst = [&](int kv) {
#pragma unroll
            for (int i = 0; i < 4; ++i) {
                const int t = wid + 4 * i, gt = qb * 8 - 8 + t; const bool ok = gt >= 0;
                const unsigned so = (unsigned)head * 65536 + (ok ? gt : 0) * 1024;
                glds<SC1>(mk_rsrc(ws + (kv ? WS_V : WS_K), ok ? 2u << 20 : 0), lds + (kv ? L_V + t * VST : L_K + t * 1024), lane * 16, so);
            } };
        u64 f;
        auto qkst = [&]() {
            CFENCE();
#pragma unroll
            for (int i = 0; i < 2; ++i) glds<SC1>(rs, lds + L_Q + (wid * 2 + i) * 1024, lane * 16, WS_Q + (unsigned)head * 65536 + (qb * 8 + wid * 2 + i) * 1024);
            kvst(0); };
        WAITV(12); BAR();
        stage(18);
        WAITV(12); BAR(); stage(19);
        WAITV(12); BAR();
        FLAG_ISSUE(f, ws + fq_off);
        WAITV(1); BAR();
        FLAG_LANDED(f, 0);
        if (!__all(f == tag)) flag_wait(ws, fq_off, tag);
        qkst();
        attn_setup(al, lds, lane, wid8);
        if (wid == 0 && lane < 32) {
            const u32x4 ones = {0x3f803f80u, 0x3f803f80u, 0x3f803f80u, 0x3f803f80u};
            *(LAS u32x4*)(lds + L_V + (lane & 15) * VST + 1024 + (lane >> 4) * 64) = ones;
            WAIT_L0();
        }
        __builtin_amdgcn_s_setprio(1);
        WAITV(0);
        FLAG_ISSUE(f, ws + fv_off);
        asm volatile("ds_add_u32 %0, %1" :: "v"(LA(lds) + L_CNT), "v"(lane == 0 ? 1u : 0u) : "memory");
#pragma unroll 1
        for (int spin = 0; spin < (1 << 22); ++spin) {
            unsigned c; asm volatile("ds_read_b32 %0, %1\n\ts_waitcnt lgkmcnt(0)" : "=v"(c) : "v"(LA(lds) + L_CNT) : "memory");
            if (__builtin_amdgcn_readfirstlane(c) == 8) break;
        }
        u64 fbv;
        attention(al, rs, head, qb * 8 + wid8, [&](int i) {
            if (i == 0) {
                FLAG_LANDED(f, 0);
                if (!__all(f == tag)) flag_wait(ws, fv_off, tag);
                CFENCE(); kvst(1);
                __builtin_amdgcn_raw_ptr_buffer_load_lds(mk_rsrc(wo, DM * DM * 2), (LAS void*)(lds + L_DUMMY2 + wid * 256), 4, (lane & 15) * 1024,
                                                         (unsigned)((tp & 7) * 64 + wid * 16) * 1024 + eb * 128, 0, 0);
            }
            else if (i == 1) { WAITV(1); BAR(); cst(0); }
            else if (i <= 4) cst(i - 1);
            else if (i == 7) FLAG_ISSUE(fbv, ws + fb_off);
        });
        {
        const rsrc_t rwo = mk_rsrc(wo, DM * DM * 2);
        const unsigned voffO = (lane >> 3) * 1024 + (((lane & 7) ^ (((lane >> 4) & 1) << 2)) << 4);
        auto ost = [&](int c) {
#pragma unroll
            for (int kk = 0; kk < 4; ++kk) glds<SC1>(rs, lds + p3_o(c) + kk * 4096 + wid * 1024, lane * 16, WS_O + (unsigned)(tb * 4 + wid) * 16384 + (c * 4 + kk) * 1024); };
        auto wst = [&](int c) {
#pragma unroll
            for (int kk = 0; kk < 4; ++kk) glds(rwo, lds + p3_w(c) + kk * 4096 + wid * 1024, voffO, (unsigned)(c * 128 + kk * 32 + wid * 8) * 1024 + eb * 128); };
        FLAG_LANDED(fbv, 1);
        if (!__all(fbv == tag)) flag_wait(ws, fb_off, tag);
        CFENCE();
        bst(0); bst(1); bst(2);
        __builtin_amdgcn_s_setprio(0);
        WAITV(8); BAR(); bst(3);
        WAITV(8); BAR(); cst(4); bst(4);
        WAITV(4 + 8); BAR(); cst(5); bst(5);
        WAITV(16); BAR();
        if (tid == 256) flag_set(ws, WS_FO + (qb * 32 + head) * 8, tag);
        cst(6); bst(6);
        WAITV(16); BAR(); cst(7); bst(7);
        WAITV(16); BAR(); cst(8); bst(8);
        WAITV(16); BAR(); cst(9); bst(9); wst(0);
        WAITV(20); BAR(); cst(10); bst(10);
        const unsigned fo_off = WS_FO + (unsigned)(tb >> 2) * 256 + (lane & 31) * 8;
        WAITV(20); BAR(); FLAG_ISSUE(f, ws + fo_off); cst(11); bst(11);
        WAITV(4 + 17); BAR(); cst(12); bst(12);
        FLAG_LANDED(f, 16);
        BAR();
        if (!__all(f == tag)) flag_wait(ws, fo_off, tag);
        CFENCE();
        cst(13); bst(13); ost(0);
        WAITV(20); BAR(); cst(14); bst(14);
        WAITV(20); BAR(); cst(15); bst(15);
        WAITV(20); BAR(); ost(1); wst(1);
        WAITV(16); BAR(); ost(2); wst(2);
        WAITV(16); BAR(); ost(3); wst(3);
        WAITV(16); BAR();
        WAITV(8); BAR();
        WAITV(0); BAR();
        }
    }
}

extern "C" void kernel_launch(void* const* d_in, const int* in_sizes, int n_in, void* d_out, int out_size, void* d_ws, size_t ws_size,
                              hipStream_t stream) {
    const u16* x = (const u16*)d_in[0]; const u16* wqkv = (const u16*)d_in[1]; const u16* wo = (const u16*)d_in[2];
    const u16* cre = (const u16*)d_in[4]; const u16* cim = (const u16*)d_in[5];
    const u16* hre = (const u16*)d_in[6]; const u16* him = (const u16*)d_in[7]; const u16* gw = (const u16*)d_in[8];
    (void)hipFuncSetAttribute((const void*)gated_mix_fused, hipFuncAttributeMaxDynamicSharedMemorySize, LDS_BYTES);
    gated_mix_fused<<<256, 512, LDS_BYTES, stream>>>(x, wqkv, wo, cre, cim, hre, him, gw, (char*)d_ws, (u16*)d_out);
}
```
